```python
import torch

def _softmax(x, axis=-1):
    return torch.softmax(x, dim=axis)
def _gelu(x):
    return torch.nn.functional.gelu(x, approximate="tanh")
def _silu(x):
    return torch.nn.functional.silu(x)
def _sigmoid(x):
    return torch.sigmoid(x)
def _layernorm(x, axis=-1, eps=1e-5):
    m = x.mean(dim=axis, keepdim=True)
    v = x.var(dim=axis, keepdim=True, unbiased=False)
    return (x - m) / torch.sqrt(v + eps)
def _scatter_add(tgt, idx, src, axis=0):
    return tgt.clone().index_add_(axis, idx, src)
def _segment_sum(src, seg, n):
    out = torch.zeros((n,) + src.shape[1:], dtype=src.dtype)
    return out.index_add_(0, seg, src)
def _pad(x, pw):
    flat = tuple(v for pair in reversed(pw) for v in pair)
    return torch.nn.functional.pad(x, flat)

kernel_name = 'interleave_layers__spectral_fnet__moe_arch_mod__prompt'

def setup_inputs():

    _w = (lambda a: (lambda t: t.to(torch.bfloat16) if t.is_floating_point() else t)(torch.as_tensor(a)))
    def _A_setup():
        B, S, D = 1, 2048, 512
        ns = torch.arange(S)[:, None] * torch.arange(S)[None, :]
        nd = torch.arange(D)[:, None] * torch.arange(D)[None, :]
        Ws_re = torch.cos(-2.0 * torch.pi * ns / S).to(float)
        Ws_im = torch.sin(-2.0 * torch.pi * ns / S).to(float)
        Wd_re = torch.cos(-2.0 * torch.pi * nd / D).to(float)
        Wd_im = torch.sin(-2.0 * torch.pi * nd / D).to(float)
        return {
            "x": _w((lambda *s: torch.randn(*s, dtype=torch.bfloat16) * 0.05)(B, S, D)),
            "dft_seq_re": _w(Ws_re),
            "dft_seq_im": _w(Ws_im),
            "dft_hid_re": _w(Wd_re),
            "dft_hid_im": _w(Wd_im),
        }
    _A_in = _A_setup()

    def _B_setup():
        B, S, D, F, C = 1, 2048, 512, 1792, 1
        return {
            "x": _w((lambda *s: torch.randn(*s, dtype=torch.bfloat16) * 0.05)(B, S, D)),
            "w_r": _w((lambda *s: torch.randn(*s, dtype=torch.bfloat16) * 1.6)(D)),
            "w_gate": _w((lambda *s: torch.randn(*s, dtype=torch.bfloat16) * 0.4)(D, F)),
            "w_up": _w((lambda *s: torch.randn(*s, dtype=torch.bfloat16) * 0.4)(D, F)),
            "w_down": _w((lambda *s: torch.randn(*s, dtype=torch.bfloat16) * 0.4)(F, D)),
        }
    _B_in = _B_setup()

    return {
        "A_x": _A_in["x"],
        "A_dft_seq_re": _A_in["dft_seq_re"],
        "A_dft_seq_im": _A_in["dft_seq_im"],
        "A_dft_hid_re": _A_in["dft_hid_re"],
        "A_dft_hid_im": _A_in["dft_hid_im"],
        "B_x": _B_in["x"],
        "B_w_r": _B_in["w_r"],
        "B_w_gate": _B_in["w_gate"],
        "B_w_up": _B_in["w_up"],
        "B_w_down": _B_in["w_down"],
    }

def reference(A_x, A_dft_seq_re, A_dft_seq_im, A_dft_hid_re, A_dft_hid_im, B_x, B_w_r, B_w_gate, B_w_up, B_w_down):
    def _A_ref(x, dft_seq_re, dft_seq_im, dft_hid_re, dft_hid_im):
        a = torch.einsum("bsd,ed->bse", x, dft_hid_re)
        b = torch.einsum("bsd,ed->bse", x, dft_hid_im)
        y = torch.einsum("ts,bse->bte", dft_seq_re, a) - torch.einsum("ts,bse->bte", dft_seq_im, b)
        return y

    def _B_ref(x, w_r, w_gate, w_up, w_down):
        B, S, D = x.shape
        C = 1
        r = x @ w_r
        idx = torch.argsort(-r, axis=-1)[:, :C]
        idx_e = (lambda x, n, axis: torch.repeat_interleave(x, n, dim=axis))(idx.reshape(B, C, 1), D, axis=2)
        x_sel = (lambda x, i, axis: torch.take_along_dim(x, i, dim=axis))(x, idx_e, axis=1)
        g = _silu(x_sel @ w_gate)
        u = x_sel @ w_up
        y = (g * u) @ w_down
        r_sel = _sigmoid((lambda x, i, axis: torch.take_along_dim(x, i, dim=axis))(r, idx, axis=1))
        y = y * r_sel.reshape(B, C, 1)
        b_off = (torch.arange(B) * S).reshape(B, 1)
        flat_idx = (idx + b_off).reshape(B * C)
        zero = (x * 0.0).reshape(B * S, D)
        upd = _scatter_add(zero, flat_idx, y.reshape(B * C, D), axis=0)
        return x + upd.reshape(B, S, D)

    x = A_x
    x = x + _A_ref(_layernorm(x, axis=-1), A_dft_seq_re, A_dft_seq_im, A_dft_hid_re, A_dft_hid_im)
    x = x + _B_ref(_layernorm(x, axis=-1), B_w_r, B_w_gate, B_w_up, B_w_down)
    x = x + _A_ref(_layernorm(x, axis=-1), A_dft_seq_re, A_dft_seq_im, A_dft_hid_re, A_dft_hid_im)
    x = x + _B_ref(_layernorm(x, axis=-1), B_w_r, B_w_gate, B_w_up, B_w_down)
    return x
```

```cpp
#include <hip/hip_runtime.h>
#include <hip/hip_bf16.h>
#include <stdint.h>

typedef short bf16x8 __attribute__((ext_vector_type(8)));
typedef float f32x4 __attribute__((ext_vector_type(4)));
typedef unsigned short u16;
constexpr int S = 2048, D = 512, F = 1792;

__device__ __forceinline__ float bf2f(u16 b) { return __uint_as_float((unsigned)b << 16); }
__device__ __forceinline__ u16 f2bf(float f) { __hip_bfloat16 h = __float2bfloat16(f); return *(u16*)&h; }
__device__ __forceinline__ float wsum(float v) { for (int o = 32; o; o >>= 1) v += __shfl_xor(v, o); return v; }

__global__ void k_ln(const float* xf, const u16* xb, u16* xn) {
  int row = blockIdx.x * 4 + (threadIdx.x >> 6), lane = threadIdx.x & 63;
  float v[8]; float s = 0;
  for (int j = 0; j < 8; j++) { v[j] = xf ? xf[row * D + lane * 8 + j] : bf2f(xb[row * D + lane * 8 + j]); s += v[j]; }
  float m = wsum(s) / D; float q = 0;
  for (int j = 0; j < 8; j++) q += (v[j] - m) * (v[j] - m);
  float rs = 1.0f / sqrtf(wsum(q) / D + 1e-5f);
  for (int j = 0; j < 8; j++) xn[row * D + lane * 8 + j] = f2bf((v[j] - m) * rs);
}
template <int MODE>
__global__ void k_gemm(const u16* A, const u16* B, int K, u16* Cb, float* Cf, const float* basef, const u16* baseb, int ldc) {
  int lane = threadIdx.x & 63, w = threadIdx.x >> 6;
  int m0 = blockIdx.y * 64 + (w >> 1) * 32, n0 = blockIdx.x * 64 + (w & 1) * 32;
  f32x4 acc[2][2] = {};
  for (int k = 0; k < K; k += 32) {
    bf16x8 a[2], b[2];
    for (int i = 0; i < 2; i++) {
      a[i] = *(const bf16x8*)(A + (size_t)(m0 + i * 16 + (lane & 15)) * K + k + (lane >> 4) * 8);
      b[i] = *(const bf16x8*)(B + (size_t)(n0 + i * 16 + (lane & 15)) * K + k + (lane >> 4) * 8);
    }
    for (int i = 0; i < 2; i++) for (int j = 0; j < 2; j++)
      acc[i][j] = __builtin_amdgcn_mfma_f32_16x16x32_bf16(a[i], b[j], acc[i][j], 0, 0, 0);
  }
  for (int i = 0; i < 2; i++) for (int j = 0; j < 2; j++) for (int r = 0; r < 4; r++) {
    int m = m0 + i * 16 + (lane >> 4) * 4 + r, n = n0 + j * 16 + (lane & 15);
    size_t o = (size_t)m * ldc + n;
    if (MODE == 0) Cb[o] = f2bf(acc[i][j][r]);
    else if (MODE == 1) Cf[o] = (basef ? basef[o] : bf2f(baseb[o])) + acc[i][j][r];
    else Cf[o] -= acc[i][j][r];
  }
}
__global__ void k_router(const float* x1, const u16* wr, float* xn, float* x2, float* r) {
  int row = blockIdx.x * 4 + (threadIdx.x >> 6), lane = threadIdx.x & 63;
  float v[8]; float s = 0;
  for (int j = 0; j < 8; j++) { v[j] = x1[row * D + lane * 8 + j]; s += v[j]; }
  float m = wsum(s) / D; float q = 0;
  for (int j = 0; j < 8; j++) q += (v[j] - m) * (v[j] - m);
  float rs = 1.0f / sqrtf(wsum(q) / D + 1e-5f); float d = 0;
  for (int j = 0; j < 8; j++) { float n = (v[j] - m) * rs; xn[row * D + lane * 8 + j] = n; x2[row * D + lane * 8 + j] = v[j] + n; d += n * bf2f(wr[lane * 8 + j]); }
  d = wsum(d); if (lane == 0) r[row] = d;
}
__device__ int argmax_r(const float* r, float* sv, int* si) {
  float bv = -INFINITY; int bi = 0;
  for (int i = threadIdx.x; i < S; i += blockDim.x) if (r[i] > bv) { bv = r[i]; bi = i; }
  sv[threadIdx.x] = bv; si[threadIdx.x] = bi; __syncthreads();
  for (int o = blockDim.x / 2; o; o >>= 1) { if (threadIdx.x < o) { float v = sv[threadIdx.x + o]; int i = si[threadIdx.x + o];
      if (v > sv[threadIdx.x] || (v == sv[threadIdx.x] && i < si[threadIdx.x])) { sv[threadIdx.x] = v; si[threadIdx.x] = i; } } __syncthreads(); }
  return si[0];
}
__global__ void k_ffn1(const float* r, const float* xn, const u16* wg, const u16* wu, float* h) {
  __shared__ float sv[256]; __shared__ int si[256];
  int idx = argmax_r(r, sv, si);
  int f = blockIdx.x * 256 + threadIdx.x; float g = 0, u = 0;
  for (int d = 0; d < D; d++) { float x = xn[idx * D + d]; g += x * bf2f(wg[d * F + f]); u += x * bf2f(wu[d * F + f]); }
  h[f] = g / (1.0f + expf(-g)) * u;
}
__global__ void k_ffn2(const float* r, const float* h, const u16* wd, float* x2) {
  __shared__ float sv[256]; __shared__ int si[256];
  int idx = argmax_r(r, sv, si);
  int e = blockIdx.x * 256 + threadIdx.x; float y = 0;
  for (int f = 0; f < F; f++) y += h[f] * bf2f(wd[f * D + e]);
  x2[idx * D + e] += y / (1.0f + expf(-r[idx]));
}
__global__ void k_out(const float* x, u16* o) { int i = blockIdx.x * 256 + threadIdx.x; o[i] = f2bf(x[i]); }

extern "C" void kernel_launch(void* const* d_in, const int* in_sizes, int n_in, void* d_out, int out_size, void* d_ws, size_t ws_size, hipStream_t stream) {
  const u16 *x0 = (const u16*)d_in[0], *wsr = (const u16*)d_in[1], *wsi = (const u16*)d_in[2], *wdr = (const u16*)d_in[3], *wdi = (const u16*)d_in[4];
  const u16 *wr = (const u16*)d_in[6], *wg = (const u16*)d_in[7], *wu = (const u16*)d_in[8], *wdn = (const u16*)d_in[9];
  char* ws = (char*)d_ws;
  u16* xn = (u16*)ws; u16* aT = (u16*)(ws + (4 << 20)); u16* bT = (u16*)(ws + (8 << 20));
  float* x1 = (float*)(ws + (12 << 20)); float* xnf = (float*)(ws + (16 << 20)); float* x2 = (float*)(ws + (20 << 20));
  float* r = (float*)(ws + (24 << 20)); float* h = (float*)(ws + (25 << 20));
  for (int l = 0; l < 2; l++) {
    k_ln<<<S / 4, 256, 0, stream>>>(l ? x2 : nullptr, x0, xn);
    k_gemm<0><<<dim3(S / 64, D / 64), 256, 0, stream>>>(wdr, xn, D, aT, nullptr, nullptr, nullptr, S);
    k_gemm<0><<<dim3(S / 64, D / 64), 256, 0, stream>>>(wdi, xn, D, bT, nullptr, nullptr, nullptr, S);
    k_gemm<1><<<dim3(D / 64, S / 64), 256, 0, stream>>>(wsr, aT, S, nullptr, x1, l ? x2 : nullptr, x0, D);
    k_gemm<2><<<dim3(D / 64, S / 64), 256, 0, stream>>>(wsi, bT, S, nullptr, x1, nullptr, nullptr, D);
    k_router<<<S / 4, 256, 0, stream>>>(x1, wr, xnf, x2, r);
    k_ffn1<<<F / 256, 256, 0, stream>>>(r, xnf, wg, wu, h);
    k_ffn2<<<D / 256, 256, 0, stream>>>(r, h, wdn, x2);
  }
  k_out<<<S * D / 256, 256, 0, stream>>>(x2, (u16*)d_out);
}
```

```cpp
#include <hip/hip_runtime.h>
#include <hip/hip_bf16.h>
#include <utility>

typedef unsigned v4u __attribute__((ext_vector_type(4)));
typedef unsigned v2u __attribute__((ext_vector_type(2)));
typedef float f32x4 __attribute__((ext_vector_type(4)));
typedef float f32x2 __attribute__((ext_vector_type(2)));
typedef __bf16 bf16x2 __attribute__((ext_vector_type(2)));
typedef unsigned short u16;
typedef __amdgpu_buffer_rsrc_t rsrc_t;
#define DI __device__ __forceinline__
#define LAS __attribute__((address_space(3)))
#define SB() __builtin_amdgcn_sched_barrier(0)
#define BAR() asm volatile("s_waitcnt lgkmcnt(0)\n\ts_barrier" ::: "memory")
#define IC(v) std::integral_constant<int, (v)>{}

#define X_NT 0
#define WD_NT 0
#define WS_NT 1
#define WS_ROT 1
#define DIAG 0
#define STAMP(i) do {} while (0)
#define STAMPC(i) do {} while (0)

namespace {
constexpr int S_ = 2048, D_ = 512, F_ = 1792;
constexpr float EPS = 1e-5f;
constexpr unsigned FLAG_OFF = 65536;
constexpr unsigned SM_OFF = 262144;
constexpr unsigned R1_OFF = SM_OFF, R2_OFF = SM_OFF + 4096, H1_OFF = SM_OFF + 8192, H2_OFF = SM_OFF + 12288, U1_OFF = SM_OFF + 16384,
                   DA_OFF = SM_OFF + 20480, XR_OFF = SM_OFF + 32768;
constexpr unsigned AB_OFF = 2u << 20, AB_SZ = 4u << 20;
constexpr unsigned Y_OFF = 10u << 20, Y_SZ = 8u << 20, YP = 2u << 20;
constexpr unsigned WS_BYTES = 28u << 20, DBG_OFF = 27u << 20;
constexpr unsigned OOB = 0x7f000000u, POISON = 0xffffffffu;
constexpr int SC1 = 16;
constexpr int SPIN_CAP = 1 << 16;
constexpr unsigned L_RED = 131072, L_MISC = 155648, L_WR = L_MISC, L_ST = L_MISC + 1024, L_FH = L_MISC + 1280, L_FD = L_MISC + 1536,
                   L_FA = L_MISC + 1792, L_CS = L_MISC + 2048, L_CSUM = L_MISC + 2560, LDS_BYTES = 163840;

template <class F, int... I> DI void sfor_(F&& f, std::integer_sequence<int, I...>) { (f(std::integral_constant<int, I>{}), ...); }
template <int N, class F> DI void sfor(F&& f) { sfor_(f, std::make_integer_sequence<int, N>{}); }
DI unsigned rfl(unsigned x) { return __builtin_amdgcn_readfirstlane(x); }
DI float asf(unsigned x) { return __builtin_bit_cast(float, x); }
DI unsigned asu(float x) { return __builtin_bit_cast(unsigned, x); }
DI float blo(unsigned d) { return asf(d << 16); }
DI float bhi(unsigned d) { return asf(d & 0xffff0000u); }
DI unsigned cvtpk(float lo, float hi) { f32x2 v = {lo, hi}; bf16x2 b = __builtin_convertvector(v, bf16x2); return __builtin_bit_cast(unsigned, b); }
DI unsigned san(unsigned d) { return min(d, 0xfffffffeu); }
DI float opq(float a) { asm("" : "+v"(a)); return a; }
DI unsigned max3u(unsigned a, unsigned b, unsigned c) { return max(max(a, b), c); }
DI unsigned maxv(unsigned m, const v4u& v) { return max3u(max3u(m, v[0], v[1]), v[2], v[3]); }
DI float dot2(unsigned a, unsigned b, float c) { float r; asm("v_dot2_f32_bf16 %0, %1, %2, %3" : "=v"(r) : "v"(a), "v"(b), "v"(c)); return r; }
DI float dot2z(unsigned a, unsigned b) { float r; asm("v_dot2_f32_bf16 %0, %1, %2, 0" : "=v"(r) : "v"(a), "v"(b)); return r; }
DI unsigned plo(unsigned a, unsigned b) { return __builtin_amdgcn_perm(b, a, 0x05040100u); }
DI unsigned phi(unsigned a, unsigned b) { return __builtin_amdgcn_perm(b, a, 0x07060302u); }
template <int C> DI float dpp(float v) { return asf(__builtin_amdgcn_update_dpp(0, asu(v), C, 0xf, 0xf, true)); }
DI float sum16(float v) { auto r = __builtin_amdgcn_permlane16_swap(asu(v), asu(v), false, false); return asf(r[0]) + asf(r[1]); }
DI float sum32(float v) { auto r = __builtin_amdgcn_permlane32_swap(asu(v), asu(v), false, false); return asf(r[0]) + asf(r[1]); }
DI float max16(float v) { auto r = __builtin_amdgcn_permlane16_swap(asu(v), asu(v), false, false); return fmaxf(asf(r[0]), asf(r[1])); }
DI float max32(float v) { auto r = __builtin_amdgcn_permlane32_swap(asu(v), asu(v), false, false); return fmaxf(asf(r[0]), asf(r[1])); }
DI float rowsum(float v) { v += dpp<0x128>(v); v += dpp<0x124>(v); v += dpp<0x122>(v); v += dpp<0x121>(v); return v; }
DI float wavesum(float v) { return sum32(sum16(rowsum(v))); }
DI float wavemax(float v) { v = fmaxf(v, dpp<0x128>(v)); v = fmaxf(v, dpp<0x124>(v)); v = fmaxf(v, dpp<0x122>(v)); v = fmaxf(v, dpp<0x121>(v)); return max32(max16(v)); }
DI float fold32(float a, float b) { auto r = __builtin_amdgcn_permlane32_swap(asu(a), asu(b), false, false); return asf(r[0]) + asf(r[1]); }
DI float fold16(float a, float b) { auto r = __builtin_amdgcn_permlane16_swap(asu(a), asu(b), false, false); return asf(r[0]) + asf(r[1]); }
DI float sigmoidf(float x) { return __builtin_amdgcn_rcpf(1.0f + __builtin_amdgcn_exp2f(-1.44269504089f * x)); }

DI void mfa0(f32x4& d, const v4u& a, const v4u& b) { asm volatile("v_mfma_f32_16x16x32_bf16 %0, %1, %2, 0" : "=&v"(d) : "v"(a), "a"(b)); }
DI void mfa1(f32x4& d, const v4u& a, const v4u& b) { asm volatile("v_mfma_f32_16x16x32_bf16 %0, %1, %2, %0" : "+v"(d) : "v"(a), "a"(b)); }
DI void mfv0(f32x4& d, const v4u& a, const v4u& b) { asm volatile("v_mfma_f32_16x16x32_bf16 %0, %1, %2, 0" : "=&v"(d) : "v"(a), "v"(b)); }
DI void mfv1(f32x4& d, const v4u& a, const v4u& b) { asm volatile("v_mfma_f32_16x16x32_bf16 %0, %1, %2, %0" : "+v"(d) : "v"(a), "v"(b)); }
template <bool FIRST> DI void mfv(f32x4& d, const v4u& a, const v4u& b) { if constexpr (FIRST) mfv0(d, a, b); else mfv1(d, a, b); }
DI v4u splat(unsigned c) { v4u v = {c, c, c, c}; asm volatile("s_nop 1" : "+v"(v)); return v; }
DI void nop2() { asm volatile("s_nop 1"); }
DI void nop8() { asm volatile("s_nop 7"); }
DI void dma(unsigned m0, unsigned voff, rsrc_t r, unsigned so) {
  asm volatile("s_mov_b32 m0, %0\n\ts_nop 0\n\tbuffer_load_dwordx4 %1, %2, %3 offen lds" :: "s"(m0), "v"(voff), "s"(r), "s"(so) : "memory"); }
DI void dma_nt(unsigned m0, unsigned voff, rsrc_t r, unsigned so) {
  asm volatile("s_mov_b32 m0, %0\n\ts_nop 0\n\tbuffer_load_dwordx4 %1, %2, %3 offen nt lds" :: "s"(m0), "v"(voff), "s"(r), "s"(so) : "memory"); }
template <bool NT> DI void dma_x(unsigned m0, unsigned voff, rsrc_t r, unsigned so) { if constexpr (NT) dma_nt(m0, voff, r, so); else dma(m0, voff, r, so); }
template <int OFF> DI void dsr_a(v4u& d, unsigned addr) { asm volatile("ds_read_b128 %0, %1 offset:%2" : "=a"(d) : "v"(addr), "i"(OFF) : "memory"); }
template <int N> DI void waitvm() { asm volatile("s_waitcnt vmcnt(%0)" :: "i"(N) : "memory"); }
DI void waitlgkm() { asm volatile("s_waitcnt lgkmcnt(0)" ::: "memory"); }
DI void st128_asm(const v4u& v, unsigned voff, rsrc_t r, unsigned so) {
  asm volatile("buffer_store_dwordx4 %0, %1, %2, %3 offen sc1\n\ts_nop 1" :: "v"(v), "v"(voff), "s"(r), "s"(so) : "memory"); }
DI void st64_asm(const v2u& v, unsigned voff, rsrc_t r) { asm volatile("buffer_store_dwordx2 %0, %1, %2, 0 offen sc1" :: "v"(v), "v"(voff), "s"(r) : "memory"); }
DI void ld64_asm(v2u& v, unsigned voff, rsrc_t r) { asm volatile("buffer_load_dwordx2 %0, %1, %2, 0 offen sc1" : "=v"(v) : "v"(voff), "s"(r) : "memory"); }
DI void ld128_asm(v4u& v, unsigned voff, rsrc_t r) { asm volatile("buffer_load_dwordx4 %0, %1, %2, 0 offen" : "=v"(v) : "v"(voff), "s"(r) : "memory"); }
DI void pin(v2u& v) { asm volatile("" : "+v"(v)); }
DI void pin(v4u& v) { asm volatile("" : "+v"(v)); }
DI v4u ld128(rsrc_t r, unsigned v, unsigned s) { return __builtin_amdgcn_raw_buffer_load_b128(r, v, s, SC1); }
DI v2u ld64(rsrc_t r, unsigned v, unsigned s) { return __builtin_amdgcn_raw_buffer_load_b64(r, v, s, SC1); }
DI unsigned ld32(rsrc_t r, unsigned v, unsigned s) { return __builtin_amdgcn_raw_buffer_load_b32(r, v, s, SC1); }
DI void st128(const v4u& d, rsrc_t r, unsigned v, unsigned s) { __builtin_amdgcn_raw_buffer_store_b128(d, r, v, s, SC1); }
DI void st64(const v2u& d, rsrc_t r, unsigned v, unsigned s) { __builtin_amdgcn_raw_buffer_store_b64(d, r, v, s, SC1); }
DI void st32(unsigned d, rsrc_t r, unsigned v, unsigned s) { __builtin_amdgcn_raw_buffer_store_b32(d, r, v, s, SC1); }
template <class T> DI T lds_ld(unsigned a) { return *(LAS T*)(size_t)a; }
template <class T> DI void lds_st(unsigned a, const T& v) { *(LAS T*)(size_t)a = v; }
DI void fence_c() { asm volatile("" ::: "memory"); }
DI rsrc_t mkrsrc(const void* p, unsigned bytes) { return __builtin_amdgcn_make_buffer_rsrc((void*)p, 0, bytes, 0x00020000); }

DI int fresh(int v) { asm volatile("" : "+v"(v)); return v; }
DI unsigned frag_addr(int lane, int par) { const int n = lane & 15, q = lane >> 4; return 128 * n + 16 * ((4 * par + q) ^ (n >> 1)); }
struct Ctx {
  rsrc_t rS;
  int lane, w, b, c, kh, tn, gA, gB, rh, sl2, xc;
  unsigned long long T;
};
DI bool anyl(bool p) { return __builtin_amdgcn_ballot_w64(p) != 0ull; }
#define RETRY_WHILE(bad, reload) do { if (__builtin_expect((bad), 0)) { int spin_ = 0; _Pragma("unroll 1") do { fence_c(); reload; } while ((bad) && ++spin_ < SPIN_CAP); } fence_c(); } while (0)
DI bool flag_bad(const Ctx& k, const v2u& f, unsigned need) { return anyl(__builtin_bit_cast(unsigned long long, f) - (k.T + need) >= 8ull - need); }
DI void flag_wait(const Ctx& k, v2u f, unsigned voff, unsigned need) { RETRY_WHILE(flag_bad(k, f, need), f = ld64(k.rS, voff, 0)); }
DI v2u flag_val(const Ctx& k, unsigned p) { return __builtin_bit_cast(v2u, k.T + p); }
DI unsigned blk_of_A(int c, int tn, int gA) { return (unsigned)(((((gA & 7) << 2) | tn) << 3) | ((gA >> 3) << 1) | c); }
DI unsigned blk_of_B(int c, int kh, int tn, int gB) { return (unsigned)(((((gB & 7) << 2) | tn) << 3) | ((gB >> 3) << 2) | (kh << 1) | c); }

DI v4u comb4(const v4u& a, const v4u& b, const v4u& c, const v4u& d) {
  v4u o;
#pragma unroll
  for (int i = 0; i < 4; ++i) o[i] = cvtpk((blo(a[i]) + blo(b[i])) + (blo(c[i]) + blo(d[i])), (bhi(a[i]) + bhi(b[i])) + (bhi(c[i]) + bhi(d[i])));
  return o;
}

template <int LAYER> DI void a_stats(const Ctx& k, const f32x4& S1, const f32x4& S2, f32x4& mean, f32x4& rstd, f32x4& sc) {
  const int e4 = k.lane & 15, rho = k.lane >> 4;
  float t = (e4 & 2) ? ((e4 & 1) ? S2[3] : S2[2]) : ((e4 & 1) ? S2[1] : S2[0]);
  t = (e4 >> 2) == rho ? t : 0.f;
  f32x4 Q;
  asm volatile("s_nop 1\n\tv_mfma_f32_16x16x4_f32 %0, %1, %2, 0\n\ts_nop 7\n\ts_nop 1" : "=&v"(Q) : "v"(t), "v"(1.0f));
#pragma unroll
  for (int r = 0; r < 4; ++r) {
    const float m = S1[r] * (1.0f / D_), var = fmaxf(Q[r] * (1.0f / D_) - m * m, 0.f), rs = __builtin_amdgcn_rsqf(var + EPS);
    mean[r] = m; rstd[r] = rs;
    float s = rs;
    if constexpr (LAYER == 1) { const float g = 1.0f + rs; s = g * __builtin_amdgcn_rsqf(var * g * g + EPS); }
    sc[r] = k.c ? -s : s;
  }
}
template <int LAYER> DI void a_emit(const Ctx& k, const f32x4 (&U)[8], const float (&cs)[8], const f32x4& mean, const f32x4& sc) {
  const int e4 = k.lane & 15, rho = k.lane >> 4;
  const unsigned voff = (unsigned)((rho & 1) * 65536 + (2 * k.gA + (k.w >> 1)) * 1024 + ((2 * (k.w & 1) + (rho >> 1)) * 16 + e4) * 16);
  const unsigned so = AB_OFF + LAYER * AB_SZ + (k.c * 4 + k.tn) * 524288;
  sfor<4>([&](auto I) { constexpr int i = I;
    unsigned pk[4];
#pragma unroll
    for (int h = 0; h < 2; ++h) {
      const f32x4 v = (U[2 * i + h] - mean * cs[2 * i + h]) * sc;
      pk[2 * h] = cvtpk(v[0], v[1]); pk[2 * h + 1] = cvtpk(v[2], v[3]);
      if constexpr (LAYER == 1 && i < 2) { pk[2 * h] = san(pk[2 * h]); pk[2 * h + 1] = san(pk[2 * h + 1]); }
    }
    auto e0 = __builtin_amdgcn_permlane16_swap(pk[0], pk[2], false, false), e1 = __builtin_amdgcn_permlane16_swap(pk[1], pk[3], false, false);
    const v4u o = {e0[0], e1[0], e0[1], e1[1]};
    if constexpr (LAYER == 0) st128_asm(o, voff, k.rS, so + i * 131072); else st128(o, k.rS, voff, so + i * 131072);
    SB();
  });
}

struct Sel { float r, m, rs; unsigned row, cu; };
DI void rec_load(const Ctx& k, v4u (&rc)[4], unsigned off) { sfor<4>([&](auto I) { rc[I] = ld128(k.rS, k.lane * 16 + I * 1024, off); }); }
DI Sel rec_argmax(const Ctx& k, v4u (&rc)[4], unsigned off) {
  RETRY_WHILE(anyl(maxv(maxv(maxv(maxv(0u, rc[0]), rc[1]), rc[2]), rc[3]) == POISON), (rec_load(k, rc, off)));
  v4u best = rc[0]; unsigned bi = 0;
  sfor<3>([&](auto I) { constexpr int i = I + 1; const bool g = asf(rc[i][0]) > asf(best[0]); best = g ? rc[i] : best; bi = g ? i : bi; });
  const float mx = wavemax(asf(best[0]));
  const unsigned long long bal = __ballot(asf(best[0]) == mx);
  const int L = bal ? __builtin_ctzll(bal) : 0;
  Sel s;
  s.r = asf(__builtin_amdgcn_readlane(best[0], L)); s.m = asf(__builtin_amdgcn_readlane(best[1], L)); s.rs = asf(__builtin_amdgcn_readlane(best[2], L));
  s.row = __builtin_amdgcn_readlane(best[3], L) & (S_ - 1); s.cu = __builtin_amdgcn_readlane(bi, L) * 64 + L;
  return s;
}
DI void ffn_h_partial(const Ctx& k, const unsigned (&wgu)[16], unsigned xpair) {
  float v[16], t[8], u[4];
  sfor<16>([&](auto I) { v[I] = dot2z(wgu[I], xpair); });
  asm volatile("s_nop 2");
  sfor<8>([&](auto I) { t[I] = fold32(v[2 * I], v[2 * I + 1]); });
  sfor<4>([&](auto I) { u[I] = rowsum(fold16(t[2 * I], t[2 * I + 1])); });
  if ((k.lane & 15) == 0) lds_st<f32x4>(L_FH + k.w * 64 + (2 * ((k.lane >> 4) & 1) + (k.lane >> 5)) * 16, f32x4{u[0], u[1], u[2], u[3]});
}
DI void ffn_h_publish(const Ctx& k, unsigned off, int fc) {
  const int f = k.lane & 7; float g = 0.f, u = 0.f;
  sfor<4>([&](auto W) { g += lds_ld<float>(L_FH + W * 64 + ((f & 3) * 4 + (f >> 2)) * 4); u += lds_ld<float>(L_FH + W * 64 + ((f & 3) * 4 + 2 + (f >> 2)) * 4); });
  const float h = g * sigmoidf(g) * u;
  const unsigned d = san(cvtpk(h, dpp<0xB1>(h)));
  if (k.lane < 8 && !(k.lane & 1) && fc >= 0) st32(d, k.rS, fc * 16 + k.lane * 2, off);
}
DI void ffn_h_load(const Ctx& k, unsigned (&hp)[4], unsigned off) {
  sfor<4>([&](auto I) { constexpr int i = I; hp[i] = ld32(k.rS, (i == 3 && k.lane >= 32) ? OOB : (unsigned)(k.w * 896 + i * 256 + k.lane * 4), off); });
}
DI void ffn_d_partial(const Ctx& k, const unsigned (&wd)[32], unsigned (&hp)[4], unsigned off) {
  RETRY_WHILE(anyl(max3u(max(hp[0], hp[1]), hp[2], hp[3]) == POISON), (ffn_h_load(k, hp, off)));
  float v[8], t[4], u[2];
  sfor<8>([&](auto I) { v[I] = dot2z(wd[I], hp[0]); });
  sfor<24>([&](auto I) { constexpr int i = I; v[i & 7] = dot2(wd[8 + i], hp[1 + (i >> 3)], v[i & 7]); });
  asm volatile("s_nop 2");
  sfor<4>([&](auto I) { t[I] = fold32(v[2 * I], v[2 * I + 1]); });
  sfor<2>([&](auto I) { u[I] = rowsum(fold16(t[2 * I], t[2 * I + 1])); });
  if ((k.lane & 15) == 0) lds_st<f32x2>(L_FD + k.w * 32 + (2 * ((k.lane >> 4) & 1) + (k.lane >> 5)) * 8, f32x2{u[0], u[1]});
}
DI float ffn_d_sum(const Ctx& k) {
  const int f = k.lane & 7; float y = 0.f;
  sfor<4>([&](auto W) { y += lds_ld<float>(L_FD + W * 32 + ((f & 3) * 2 + (f >> 2)) * 4); });
  return y;
}
DI unsigned yoff(unsigned row, unsigned ch) { return (((row >> 4) * 16 + (ch >> 2)) * 64 + (ch & 3) * 16 + (row & 15)) * 16; }

struct Params { const u16 *x0, *wsr, *wsi, *wdr, *wdi, *wr, *wg, *wu, *wdn; u16* out; char* ws; };

__global__ void __launch_bounds__(256, 1) fnet_mod_kernel(Params P) {
  extern __shared__ __attribute__((aligned(16))) char smem[];
  STAMP(0);
  Ctx k;
  k.b = blockIdx.x; k.lane = threadIdx.x & 63; k.w = rfl(threadIdx.x >> 6);
  k.xc = k.b & 7; k.sl2 = k.b >> 5; k.c = k.xc & 1; k.kh = (k.xc >> 1) & 1; k.tn = (k.b >> 3) & 3;
  k.gB = (k.xc >> 2) * 8 + k.sl2; k.gA = (k.xc >> 1) * 8 + k.sl2; k.rh = k.sl2 & 3;
  k.rS = mkrsrc(P.ws, WS_BYTES);
  const int w = k.w;
  const rsrc_t rX = mkrsrc(P.x0, S_ * D_ * 2), rWr = mkrsrc(P.wr, D_ * 2);
  float swr;
  v4u Wa[64];
  {
    k.lane = fresh(k.lane); const int lane = k.lane, n16 = lane & 15, q4 = lane >> 4; const unsigned vL = lane * 16;
    const unsigned fr0 = frag_addr(lane, 0), fr1 = frag_addr(lane, 1);
    const rsrc_t rWd = mkrsrc(k.c ? P.wdi : P.wdr, D_ * D_ * 2);
    const rsrc_t rW = mkrsrc(k.c ? P.wsi : P.wsr, S_ * S_ * 2);
    v2u ep; ld64_asm(ep, 0u, k.rS);
    const unsigned ring = L_RED + w * 8192;
    unsigned vx[4];
    sfor<4>([&](auto I) { vx[I] = q4 * 1024 + (n16 ^ (4 * I + q4)) * 16; });
    const unsigned sw0 = ((lane & 7) ^ (lane >> 4)) * 16, sw1 = ((lane & 7) ^ (4 + (lane >> 4))) * 16;
    const unsigned vw0 = (lane >> 3) * 1024 + sw0, vw1 = (8 + (lane >> 3)) * 1024 + sw1, vs0 = (lane >> 3) * 4096 + sw0, vs1 = (8 + (lane >> 3)) * 4096 + sw1;
    auto xstage = [&](int q) { return q < 2 ? 98304 + q * 16384 + w * 4096 : ring + (q - 2) * 4096; };
    auto piece = [&](auto N) { constexpr int n = N;
      constexpr bool isx = n < 4 || (n >= 12 && n < 16) || (n >= 24 && n < 32);
      if constexpr (isx) { constexpr int q = n < 4 ? 0 : n < 16 ? 1 : n < 28 ? 2 : 3, i = n & 3;
        dma_x<X_NT>(xstage(q) + i * 1024, vx[i], rX, (64 * k.gA + 16 * w + 4 * i) * 1024 + (q ^ k.rh) * 256);
      } else { constexpr int q = n < 12 ? 0 : n < 24 ? 1 : n < 40 ? 2 : 3, i = (n < 12 ? n - 4 : n) & 7;
        const unsigned nt = 2 * w + ((i >> 1) & 1);
        dma_x<(WD_NT == 2 || (WD_NT == 1 && q == 0))>(q * 32768 + (i >> 2) * 16384 + nt * 2048 + (i & 1) * 1024, (i & 1) ? vw1 : vw0, rWd, (128 * k.tn + 16 * nt) * 1024 + (q ^ k.rh) * 256 + (i >> 2) * 128);
      } };
    const unsigned sb = (128 * k.gB) * 4096 + (1024 * k.kh + 256 * w) * 2, tx = (2 * w) ^ (WS_ROT ? k.tn : 0);
    auto ws_piece = [&](auto I) { constexpr int i = I;
      dma_x<(WS_NT == 2 || (WS_NT == 1 && ((i >> 3) & 3) == 0))>(ring + (i & 7) * 1024, (i & 1) ? vs1 : vs0, rW, sb + ((i >> 3) ^ tx) * 65536 + ((i >> 1) & 3) * 128); };
    sfor<20>([&](auto N) { piece(N); });
    v4u xf[16];
    unsigned xb[4];
    sfor<4>([&](auto Pp) { xb[Pp] = n16 * 256 + ((4 * Pp + q4) ^ n16) * 16; });
    auto rdx = [&](auto Q) { constexpr int q = Q; sfor<4>([&](auto Pp) { xf[4 * q + Pp] = lds_ld<v4u>(xstage(q) + xb[Pp]); }); };
    waitvm<8>();
    pin(ep);
    k.T = (((unsigned long long)rfl(ep[1]) << 32) | rfl(ep[0])) + 8ull;
    rdx(IC(0));
    BAR();
    STAMP(1);
    f32x4 U[8], S1, S2, C0, C1;
    const v4u ones = splat(0x3f803f80u);
    v4u wrv, wf[10];
    unsigned fb[2][2] = {{fr0, fr1}, {fr0 + 65536, fr1 + 65536}}, cb[2][2];
    sfor<4>([&](auto I) { constexpr int i = I; fb[i >> 1][i & 1] = fresh(fb[i >> 1][i & 1]); cb[i >> 1][i & 1] = fresh(fb[i >> 1][i & 1] + w * 4096); });
    auto rdw = [&](auto I, auto N) { constexpr int i = I, nn = N, q = i >> 2, p = i & 3;
      if constexpr (nn < 8) wf[nn] = lds_ld<v4u>(fb[q >> 1][p & 1] + ((q & 1) * 32768 + (p >> 1) * 16384 + nn * 2048));
      else wf[nn] = lds_ld<v4u>(cb[q >> 1][p & 1] + ((q & 1) * 32768 + (p >> 1) * 16384 + (nn - 8) * 2048)); };
    sfor<10>([&](auto N) { rdw(IC(0), N); });
    sfor<16>([&](auto I) { constexpr int i = I; constexpr bool first = i == 0;
      if constexpr (i == 3) { waitvm<8>(); rdx(IC(1)); BAR(); }
      if constexpr (i == 7) { waitvm<8>(); rdx(IC(2)); rdx(IC(3)); BAR(); }
      if constexpr (i == 11) { waitvm<7>(); asm volatile("s_barrier" ::: "memory"); }
      auto issue = [&](auto J) { constexpr int j = J;
        if constexpr (i < 7) piece(IC(20 + 4 * i + j)); else if constexpr (i < 11 && (j & 1) == 0 && 2 * (i - 7) + (j >> 1) < 7) ws_piece(IC(2 * (i - 7) + (j >> 1))); };
      SB();
      mfv<first>(S1, xf[i], ones); mfv<first>(S2, xf[i], xf[i]);
      issue(IC(0));
      sfor<8>([&](auto N) { mfv<first>(U[N], xf[i], wf[N]); SB(); if constexpr (i < 15) rdw(IC(i + 1), N); if constexpr (N == 2) issue(IC(1)); if constexpr (N == 5) issue(IC(2)); SB(); });
      mfv<first>(C0, ones, wf[8]); SB(); if constexpr (i < 15) rdw(IC(i + 1), IC(8)); issue(IC(3)); SB();
      mfv<first>(C1, ones, wf[9]); SB(); if constexpr (i < 15) rdw(IC(i + 1), IC(9)); SB();
      if constexpr (i == 12) {
        st128_asm(splat(POISON), w == 0 ? XR_OFF + k.b * 1024 + vL : (w == 1 && lane < 8) ? SM_OFF + k.b * 128 + vL : OOB, k.rS, 0);
        ld128_asm(wrv, vL, rWr);
      }
      if constexpr (DIAG >= 2 && (i & 3) == 3) STAMPC(32 + (i >> 2));
    });
    nop8();
    if (lane < 16) sfor<4>([&](auto W) { lds_st<float>(L_RED + W * 8192 + 7168 + (w * 32 + lane) * 4, C0[0]); lds_st<float>(L_RED + W * 8192 + 7168 + (w * 32 + 16 + lane) * 4, C1[0]); });
    BAR();
    float cs[8];
    sfor<8>([&](auto N) { cs[N] = lds_ld<float>(ring + 7168 + (N * 16 + n16) * 4); });
    waitlgkm();
    ws_piece(IC(7));
    const unsigned rd0 = ring + fr0, rd1 = ring + fr1;
    auto batch = [&](auto B) { constexpr int bt = B;
      waitvm<(bt == 15 ? 0 : bt == 0 ? 6 : (bt == 2 || bt == 3) ? 8 : (bt == 5 || bt == 6) ? 5 : 4)>();
      sfor<4>([&](auto J) { constexpr int i = bt * 4 + J; dsr_a<((i & 7) >> 1) * 2048>(Wa[i], (i & 1) ? rd1 : rd0); });
      waitlgkm();
      if constexpr (DIAG >= 2 && (bt & 1)) STAMPC(44 + (bt >> 1));
      if constexpr (bt < 14) sfor<4>([&](auto J) { ws_piece(IC(bt * 4 + 8 + J)); }); };
    f32x4 mean, rstd, sc;
    batch(IC(0));
    a_stats<0>(k, S1, S2, mean, rstd, sc);
    SB();
    batch(IC(1));
    a_emit<0>(k, U, cs, mean, sc);
    STAMP(2);
    sfor<14>([&](auto B) { constexpr int bt = B + 2;
      batch(IC(bt));
      if constexpr (bt == 4) {
        st64_asm(flag_val(k, 1), lane == 0 ? FLAG_OFF + (k.b * 4 + w) * 128 : OOB, k.rS);
        pin(wrv);
        float s = 0.f;
#pragma unroll
        for (int i = 0; i < 4; ++i) s += blo(wrv[i]) + bhi(wrv[i]);
        swr = wavesum(s);
      }
    });
    BAR();
    if (w == 0) lds_st<v4u>(L_WR + ((((lane >> 4) ^ k.rh) * 4 + ((lane >> 2) & 3)) * 4 + (lane & 3)) * 16, wrv);
    if (w == 1 && lane < 16) { lds_st<f32x4>(L_CSUM + lane * 32, f32x4{cs[0], cs[1], cs[2], cs[3]}); lds_st<f32x4>(L_CSUM + lane * 32 + 16, f32x4{cs[4], cs[5], cs[6], cs[7]}); }
  }
  STAMP(3);
  auto flagA = [&](int n16) { return FLAG_OFF + (blk_of_A(k.c, k.tn, 16 * k.kh + 4 * w + (n16 >> 2)) * 4 + (n16 & 3)) * 128; };
  auto flagY = [&](int n16, int rw) { return FLAG_OFF + (blk_of_B(n16 & 1, (n16 >> 1) & 1, n16 >> 2, k.gA >> 1) * 4 + (((4 * (k.gA & 1) + rw) ^ (WS_ROT ? n16 >> 2 : 0)) >> 1)) * 128; };
  auto flagMe = [&](int lane) { return lane == 0 ? FLAG_OFF + (k.b * 4 + w) * 128 : OOB; };
  const int jj = k.c * 4 + k.tn, crow0 = 64 * k.gA + 8 * jj;
  const int fc = k.sl2 < 7 ? (k.sl2 * 8 + k.xc) * 4 + k.tn : -1;
  const int dc = k.sl2 >= 6 ? (k.sl2 - 6) * 32 + k.xc * 4 + k.tn : -1;
  const int an = k.sl2 < 2 ? k.sl2 * 4 + (k.xc >> 1) : -1;

  v4u ring[16];
  unsigned wgu[16], wdn[32];
  v4u graw[4];
  Sel s1;
  unsigned wcol0, wcol1;
  auto phaseB = [&](auto LAYER) { constexpr int L = LAYER;
    k.lane = fresh(k.lane); const int lane = k.lane, n16 = lane & 15, q4 = lane >> 4; const unsigned vL = lane * 16;
    const unsigned vFA = fresh(flagA(n16)), vFY = fresh(flagY(n16, L == 0 ? w : jj >> 1)), vFme = fresh(flagMe(lane));
    const unsigned vN = L == 0 ? vL : fresh(yoff(crow0 + (lane & 7), 16 * w + (lane >> 3)));
    const unsigned abS = AB_OFF + L * AB_SZ + (k.c * 4 + k.tn) * 524288 + (32 * k.kh + 8 * w) * 1024;
    const unsigned tile = (q4 & 1) ^ (2 * w) ^ (WS_ROT ? k.tn : 0);
    const unsigned vY = ((8 * k.gB + tile) * 1024 + (q4 >> 1) * 16 + n16) * 16 + 4 * k.tn * 1024;
    const unsigned yS = Y_OFF + (k.c * 2 + k.kh) * YP;
    const unsigned vL1 = vL + 1024, vL2 = vL + 2048, vL3 = vL + 3072;
    auto lda = [&](int ns, auto K) { constexpr int kk = K; return ld128(k.rS, (kk & 3) == 0 ? vL : (kk & 3) == 1 ? vL1 : (kk & 3) == 2 ? vL2 : vL3, abS + ns * 65536 + (kk >> 2) * 4096); };
    f32x4 acc[8], own[2][2], part[6]; float sm[8]; unsigned pk[4];
    v2u xa[2][2], fl; v4u rc[4], wraw[8], dy[4], du[2]; unsigned xr[4], hp[4], aold = 0;
    const rsrc_t rG = mkrsrc(P.wdn, D_ * F_ * 2);
    unsigned vG = (448 * w + 2 * lane) * (D_ * 2) + dc * 16;
    unsigned vG2 = lane < 32 ? vG : OOB, vA2 = (q4 & 1) * 65536 + (2 * k.gA + (w >> 1)) * 1024 + ((2 * (w & 1) + (q4 >> 1)) * 16 + n16) * 16;
    asm volatile("" : "+v"(vG), "+v"(vG2), "+v"(vA2));
    v4u pz;
    if constexpr (L == 0) {
      pz = splat(POISON);
      const int pm = k.c * 2 + k.kh;
      sfor<4>([&](auto I) { constexpr int i = I;
        xa[i >> 1][i & 1] = __builtin_amdgcn_raw_buffer_load_b64(rX, (128 * k.gB + 16 * ((i & 1) ^ (2 * w) ^ (WS_ROT ? k.tn : 0)) + n16) * 1024 + (128 * k.tn + 16 * (pm + 4 * (i >> 1)) + 4 * q4) * 2, 0, 0); });
      flag_wait(k, ld64(k.rS, vFA, 0), vFA, 1);
      sfor<12>([&](auto I) { constexpr int i = I; ring[i] = lda(i >> 3, IC(i & 7)); SB(); });
    } else {
      sfor<12>([&](auto I) { constexpr int i = I; ring[i] = lda(i >> 3, IC(i & 7)); SB(); });
      rec_load(k, rc, R1_OFF);
      auto bad12 = [&]() { unsigned m = 0; sfor<12>([&](auto I) { m = maxv(m, ring[I]); }); return anyl(m == POISON); };
      RETRY_WHILE(bad12(), (sfor<12>([&](auto I) { constexpr int i = I; ring[i] = lda(i >> 3, IC(i & 7)); })));
      st64(flag_val(k, 6), k.rS, vFme, 0);
      SB();
    }
    STAMP(L == 0 ? 4 : 8);
    const unsigned wa1 = L_RED + ((w ^ 1) * 6 + 0) * 1024 + vL, wa2 = L_RED + ((w ^ 2) * 6 + 2) * 1024 + vL, wa3 = L_RED + ((w ^ 3) * 6 + 4) * 1024 + vL, ra = L_RED + w * 6144 + vL;
    auto dump = [&](auto T) { constexpr int tt = T; lds_st<f32x4>(((tt >> 1) == 1 ? wa1 : (tt >> 1) == 2 ? wa2 : wa3) + (tt & 1) * 1024, acc[tt]); };
    auto fetch1 = [&](auto I) { constexpr int i = I; part[i] = lds_ld<f32x4>(ra + i * 1024); };
    auto add1 = [&](auto J, auto HP) { constexpr int j = J, v = j & 7, st = j >> 3, hq = HP;
      if constexpr (st == 0) sm[v] = own[hq][v >> 2][v & 3] + part[v >> 2][v & 3];
      else if constexpr (st == 1) sm[v] = sm[v] + part[2 + (v >> 2)][v & 3];
      else sm[v] = opq(sm[v] + part[4 + (v >> 2)][v & 3]); };
    auto addx = [&](auto NS) { constexpr int ns = NS;
      if constexpr (L == 0) if ((ns & 3) == k.c * 2 + k.kh)
        sfor<4>([&](auto I) { constexpr int i = I; const unsigned d = xa[ns >> 2][i >> 1][i & 1]; sm[2 * i] = opq(sm[2 * i] + blo(d)); sm[2 * i + 1] = opq(sm[2 * i + 1] + bhi(d)); }); };
    auto sty = [&](int ns) {
      auto e0 = __builtin_amdgcn_permlane16_swap(pk[0], pk[2], false, false), e1 = __builtin_amdgcn_permlane16_swap(pk[1], pk[3], false, false);
      st128(v4u{e0[0], e1[0], e0[1], e1[1]}, k.rS, vY, yS + L * Y_SZ + (ns >> 1) * 1024 + (ns & 1) * 512); };
    auto next_ld = [&](auto X, auto J) { constexpr int x = X, j = J;
      if constexpr (L == 0) return ld128(k.rS, vN, Y_OFF + j * YP + ((4 * k.gA + w) * 16 + 4 * (x ^ k.rh)) * 1024);
      else return ld128(k.rS, vN, (x < 2 ? Y_OFF + x * 2048 : Y_OFF + Y_SZ) + j * YP); };
    sfor<8>([&](auto NS) { constexpr int ns = NS, h = ns & 1;
      sfor<64>([&](auto G) { constexpr int g = G, tt = (g >> 2) & 7, kk = (g >> 5) * 4 + (g & 3);
        f32x4& d = tt < 2 ? own[h][tt & 1] : acc[tt];
        if constexpr (kk == 0) mfa0(d, ring[h * 8], Wa[tt * 8]); else mfa1(d, ring[h * 8 + kk], Wa[tt * 8 + kk]);
        SB();
        if constexpr ((g & 7) == 5) { constexpr int j = (g >> 3) & 3;
          if constexpr (g < 32) { if constexpr (ns < 7) ring[(1 - h) * 8 + 4 + j] = lda(ns + 1, IC(4 + j)); else ring[4 + j] = next_ld(IC(1), IC(j)); }
          else { if constexpr (ns < 6) ring[h * 8 + j] = lda(ns + 2, IC(j)); else ring[h * 8 + j] = next_ld(IC(ns == 6 ? 0 : 2), IC(j)); } }
        if constexpr (g >= 51 && (g & 3) == 3) dump(IC((g - 51) / 4 + 2));
        if constexpr (ns > 0) {
          if constexpr (g == 3) dump(IC(6));
          if constexpr (g == 7) dump(IC(7));
          if constexpr (g == 14) BAR();
          if constexpr (g >= 15 && g < 21) fetch1(IC(g - 15));
          if constexpr (g >= 26 && g < 50) add1(IC(g - 26), IC(1 - h));
          if constexpr (g == 50) addx(IC(ns - 1));
          if constexpr (g >= 51 && g < 59) { constexpr int i = (g - 51) >> 1; if constexpr (((g - 51) & 1) == 0) pk[i] = cvtpk(sm[2 * i], sm[2 * i + 1]); else pk[i] = san(pk[i]); }
          if constexpr (g == 44) BAR();
          if constexpr (g == 62) sty(ns - 1);
        }
        if constexpr (L == 0) {
          if constexpr (ns == 0 && (g == 30 || g == 62)) st128(pz, k.rS, vA2, AB_OFF + AB_SZ + (k.c * 4 + k.tn) * 524288 + (g == 62) * 131072);
          if constexpr (g == 50 && (ns & 1) && ns >= 3) { waitvm<6>(); st64(flag_val(k, (ns - 3) / 2 + 2), k.rS, vFme, 0);
          }
          if constexpr (g == 52 && (ns == 5 || ns == 7)) fl = ld64(k.rS, vFY, 0);
          if constexpr (g == 30 && ns == 6) flag_wait(k, fl, vFY, 2);
        } else {
          if constexpr (g == 52 && ns == 1) fl = ld64(k.rS, vFA, 0);
          if constexpr (g == 30 && ns == 2) flag_wait(k, fl, vFA, 6);
          if constexpr (g == 50 && ns == 5) { waitvm<6>(); st64(flag_val(k, 7), k.rS, vFme, 0); }
          if constexpr (g == 52 && ns == 6) fl = ld64(k.rS, vFY, 0);
          if constexpr (g == 30 && ns == 7) flag_wait(k, fl, vFY, 7);
          if constexpr (g == 46 && ns < 4) {
            if (__builtin_expect(anyl(__builtin_isunordered(own[h][0][0], own[h][0][1]) || __builtin_isunordered(own[h][0][2], own[h][0][3])), 0)) {
              _Pragma("unroll 1") for (int spin = 0; spin < SPIN_CAP; ++spin) {
                fence_c(); unsigned m = 0;
                sfor<8>([&](auto I) { ring[h * 8 + I] = lda(ns, I); });
                sfor<8>([&](auto I) { m = maxv(m, ring[h * 8 + I]); });
                if (!anyl(m == POISON)) break;
              }
              nop8();
              sfor<47>([&](auto G2) { constexpr int g2 = G2, t2 = (g2 >> 2) & 7, k2 = (g2 >> 5) * 4 + (g2 & 3);
                f32x4& d2 = t2 < 2 ? own[h][t2 & 1] : acc[t2];
                if constexpr (k2 == 0) mfa0(d2, ring[h * 8], Wa[t2 * 8]); else mfa1(d2, ring[h * 8 + k2], Wa[t2 * 8 + k2]); });
              sfor<2>([&](auto J) { ring[h * 8 + J] = lda(ns + 2, J); });
            }
          }
          if constexpr ((g & 31) == 1 && ns < 4) { constexpr int i = ns * 2 + (g >> 5);
            if (dc >= 0) wraw[i] = __builtin_amdgcn_raw_buffer_load_b128(rG, i < 6 ? vG : vG2, (128 * (i >> 1) + (i & 1)) * (D_ * 2), 0); }
          if constexpr (ns == 0 && g >= 8 && g < 16) { constexpr int i = g - 8;
            wgu[(i >> 2) * 8 + 2 * (i & 3)] = plo(graw[2 * (i >> 2)][i & 3], graw[2 * (i >> 2) + 1][i & 3]);
            wgu[(i >> 2) * 8 + 2 * (i & 3) + 1] = phi(graw[2 * (i >> 2)][i & 3], graw[2 * (i >> 2) + 1][i & 3]); }
          if constexpr (g == 22) {
            if constexpr (ns == 2) if (w == 0) ffn_h_publish(k, H1_OFF, fc);
            if constexpr (ns == 5) if (w == 0 && dc >= 0) { const float y = ffn_d_sum(k) * sigmoidf(s1.r); if (lane < 8) st32(san(asu(y)), k.rS, (dc * 8 + lane) * 4, U1_OFF); }
          }
          if constexpr (g == 50) {
            if constexpr (ns == 7) if (w == 0 && an >= 0) {
              float a = 0.f; sfor<4>([&](auto W) { a += lds_ld<float>(L_FA + W * 64 + n16 * 4); });
              if (lane < 16) st32(san(asu((k.c ? -a : a) - blo(aold))), k.rS, (k.c * 512 + 128 * k.tn + 16 * an + lane) * 4, DA_OFF); }
          }
          if constexpr (g == 63) {
            if constexpr (ns == 0) {
              s1 = rec_argmax(k, rc, R1_OFF);
              sfor<4>([&](auto I) { xr[I] = ld32(k.rS, yoff(s1.row, 16 * w + (lane >> 2)) + (lane & 3) * 4, Y_OFF + I * YP); });
              wcol0 = __builtin_amdgcn_raw_buffer_load_b16(mkrsrc(P.wsr, S_ * S_ * 2), ((crow0 + (lane & 7)) * S_ + s1.row) * 2, 0, 0);
              wcol1 = __builtin_amdgcn_raw_buffer_load_b16(mkrsrc(P.wsi, S_ * S_ * 2), ((crow0 + (lane & 7)) * S_ + s1.row) * 2, 0, 0);
            }
            if constexpr (ns == 1) {
              const float x0v = blo(cvtpk((blo(xr[0]) + blo(xr[1])) + (blo(xr[2]) + blo(xr[3])), 0.f)), x1v = blo(cvtpk((bhi(xr[0]) + bhi(xr[1])) + (bhi(xr[2]) + bhi(xr[3])), 0.f));
              ffn_h_partial(k, wgu, cvtpk((x0v - s1.m) * s1.rs, (x1v - s1.m) * s1.rs));
            }
            if constexpr (ns == 3) if (dc >= 0) ffn_h_load(k, hp, H1_OFF);
            if constexpr (ns == 4) if (dc >= 0) {
              sfor<16>([&](auto I) { constexpr int i = I; wdn[(i >> 2) * 8 + 2 * (i & 3)] = plo(wraw[2 * (i >> 2)][i & 3], wraw[2 * (i >> 2) + 1][i & 3]);
                                                          wdn[(i >> 2) * 8 + 2 * (i & 3) + 1] = phi(wraw[2 * (i >> 2)][i & 3], wraw[2 * (i >> 2) + 1][i & 3]); });
              ffn_d_partial(k, wdn, hp, H1_OFF);
            }
            if constexpr (ns == 6) if (an >= 0) {
              const unsigned ch = (4 * ((n16 >> 2) ^ k.rh) + (n16 & 3)) * 4 + q4;
              sfor<4>([&](auto I) { dy[I] = ld128(k.rS, yoff(s1.row, ch), Y_OFF + I * YP); });
              sfor<2>([&](auto I) { du[I] = ld128(k.rS, ch * 32 + I * 16, U1_OFF); });
              aold = __builtin_amdgcn_raw_buffer_load_b16(k.rS, ((an * 64 + (s1.row >> 5)) * 64 + ((s1.row >> 3) & 3) * 16 + n16) * 16 + (s1.row & 7) * 2,
                                                          AB_OFF + AB_SZ + (k.c * 4 + k.tn) * 524288, SC1);
            }
          }
          if constexpr (g == 40) {
            if constexpr (ns == 7) if (an >= 0) {
              const unsigned ch = (4 * ((n16 >> 2) ^ k.rh) + (n16 & 3)) * 4 + q4;
              RETRY_WHILE(anyl(maxv(maxv(0u, du[0]), du[1]) == POISON), (sfor<2>([&](auto I) { du[I] = ld128(k.rS, ch * 32 + I * 16, U1_OFF); })));
              const v4u x1 = comb4(dy[0], dy[1], dy[2], dy[3]);
              float x2[8], s = 0.f, qq = 0.f;
#pragma unroll
              for (int i = 0; i < 8; ++i) { const float v = (i & 1) ? bhi(x1[i >> 1]) : blo(x1[i >> 1]); x2[i] = v + (v - s1.m) * s1.rs + asf(du[i >> 2][i & 3]); s += x2[i]; }
              const float m2 = wavesum(s) * (1.0f / D_);
#pragma unroll
              for (int i = 0; i < 8; ++i) { x2[i] -= m2; qq += x2[i] * x2[i]; }
              const float r2 = __builtin_amdgcn_rsqf(wavesum(qq) * (1.0f / D_) + EPS);
              v4u xn; f32x4 dacc[4]; v4u wf[4];
#pragma unroll
              for (int i = 0; i < 4; ++i) xn[i] = cvtpk(x2[2 * i] * r2, x2[2 * i + 1] * r2);
              sfor<4>([&](auto M) { constexpr int m = M; wf[m] = lds_ld<v4u>(frag_addr(lane, m & 1) + w * 32768 + (m >> 1) * 16384 + an * 2048); });
              SB(); nop2();
              sfor<4>([&](auto M) { mfv0(dacc[M], xn, wf[M]); });
              nop8(); SB();
              if (q4 == w) lds_st<float>(L_FA + w * 64 + n16 * 4, (dacc[0][0] + dacc[1][1]) + (dacc[2][2] + dacc[3][3]));
            }
          }
        }
        SB();
      });
      if constexpr (DIAG >= 2) STAMPC(16 + L * 8 + ns);
    });
    if constexpr (L == 0) {
      flag_wait(k, fl, vFY, 3);
      sfor<4>([&](auto J) { ring[12 + J] = next_ld(IC(3), J); });
    }
    SB(); nop8();
    dump(IC(6)); dump(IC(7));
    BAR();
    sfor<6>([&](auto I) { fetch1(I); });
    sfor<24>([&](auto J) { add1(J, IC(1)); });
    addx(IC(7));
    sfor<4>([&](auto I) { pk[I] = san(cvtpk(sm[2 * I], sm[2 * I + 1])); });
    sty(7);
    SB();
  };

  phaseB(IC(0));
  STAMP(5);
  {
    k.lane = fresh(k.lane); const int lane = k.lane, n16 = lane & 15, q4 = lane >> 4; const unsigned vL = lane * 16;
    const unsigned vFA = fresh(flagA(n16)), vFY = fresh(flagY(n16, w)), vFme = fresh(flagMe(lane));
    f32x4 U[8], S1, S2, S3;
    const v4u ones = splat(0x3f803f80u);
    v4u yb[16][4];
    sfor<16>([&](auto I) { constexpr int i = I; yb[i >> 2][i & 3] = ring[i]; });
    const unsigned yrow = Y_OFF + (4 * k.gA + w) * 16384;
    auto ldy = [&](auto I) { constexpr int i = I; sfor<4>([&](auto J) { yb[i][J] = ld128(k.rS, vL, yrow + J * YP + (4 * ((i & 3) ^ k.rh) + (i >> 2)) * 1024); }); };
    v2u fl, fa;
    fl = ld64(k.rS, vFY, 0);
    SB();
    v4u wf[9];
    unsigned fb[2][2]; const unsigned wrb = fresh(L_WR + q4 * 16);
    sfor<4>([&](auto I) { constexpr int i = I; fb[i >> 1][i & 1] = fresh(frag_addr(lane, i & 1) + (i >> 1) * 65536); });
    auto rdw = [&](auto I, auto N) { constexpr int i = I, nn = N, p = i >> 2, q = i & 3;
      if constexpr (nn < 8) wf[nn] = lds_ld<v4u>(fb[q >> 1][p & 1] + ((q & 1) * 32768 + (p >> 1) * 16384 + nn * 2048));
      else wf[8] = lds_ld<v4u>(wrb + (q * 4 + p) * 64); };
    sfor<9>([&](auto N) { rdw(IC(0), N); });
    const unsigned vG = fresh(fc >= 0 ? (128 * w + 2 * lane) * (F_ * 2) + fc * 16 : OOB);
    const unsigned vYp = ((8 * k.gB + ((q4 & 1) ^ (2 * w) ^ (WS_ROT ? k.tn : 0))) * 1024 + (q4 >> 1) * 16 + n16) * 16 + 4 * k.tn * 1024;
    sfor<16>([&](auto I) { constexpr int i = I; constexpr bool first = i == 0;
      const v4u x1 = comb4(yb[i][0], yb[i][1], yb[i][2], yb[i][3]);
      SB();
      if constexpr (i == 2) { waitvm<9>(); st64(flag_val(k, 5), k.rS, vFme, 0); }
      if constexpr (i == 4) flag_wait(k, fl, vFY, 4);
      if constexpr (i == 5) fl = ld64(k.rS, vFY, 0);
      if constexpr (i == 8) flag_wait(k, fl, vFY, 5);
      if constexpr (i == 9) fa = ld64(k.rS, vFA, 0);
      if constexpr (i + 4 < 16) ldy(IC(i + 4));
      if constexpr (i >= 8 && i < 12) st128(splat(POISON), k.rS, vYp, Y_OFF + Y_SZ + (k.c * 2 + k.kh) * YP + 2048 + (i - 8) * 512);
      if constexpr (i >= 12) graw[i - 12] = __builtin_amdgcn_raw_buffer_load_b128(mkrsrc(i < 14 ? P.wg : P.wu, D_ * F_ * 2), vG, (i & 1) * (F_ * 2), 0);
      SB(); nop2();
      mfv<first>(S1, x1, ones); mfv<first>(S2, x1, x1); mfv<first>(S3, x1, wf[8]); SB(); if constexpr (i < 15) rdw(IC(i + 1), IC(8)); SB();
      sfor<8>([&](auto N) { mfv<first>(U[N], x1, wf[N]); SB(); if constexpr (i < 15) rdw(IC(i + 1), N); SB(); });
      if constexpr (DIAG >= 2 && (i & 1)) STAMPC(36 + (i >> 1));
    });
    STAMP(6);
    float cs[8];
    { const f32x4 c0 = lds_ld<f32x4>(L_CSUM + n16 * 32), c1 = lds_ld<f32x4>(L_CSUM + n16 * 32 + 16);
#pragma unroll
      for (int i = 0; i < 4; ++i) { cs[i] = c0[i]; cs[4 + i] = c1[i]; } }
    nop8();
    f32x4 mean, rstd, sc;
    a_stats<1>(k, S1, S2, mean, rstd, sc);
    a_emit<1>(k, U, cs, mean, sc);
    if (w == (jj >> 1)) {
      float br = -__builtin_inff(), bm = mean[0], bs = rstd[0]; unsigned bi = 0;
#pragma unroll
      for (int r = 0; r < 4; ++r) { const float rv = rstd[r] * (S3[r] - mean[r] * swr); const bool g = rv > br; br = g ? rv : br; bm = g ? mean[r] : bm; bs = g ? rstd[r] : bs; bi = g ? r : bi; }
      const int l0 = (jj & 1) * 32;
      const int ls = asf(__builtin_amdgcn_readlane(asu(br), l0 + 16)) > asf(__builtin_amdgcn_readlane(asu(br), l0)) ? l0 + 16 : l0;
      if (lane == ls) st128(v4u{san(asu(br)), san(asu(bm)), san(asu(bs)), (unsigned)(64 * k.gA + 16 * w + 4 * q4 + bi)}, k.rS, k.b * 16, R1_OFF);
      if (n16 == 0 && (q4 >> 1) == (jj & 1)) { lds_st<f32x4>(L_ST + (q4 & 1) * 16, mean); lds_st<f32x4>(L_ST + 32 + (q4 & 1) * 16, rstd); }
    }
    flag_wait(k, fa, vFA, 5);
  }
  STAMP(7);
  phaseB(IC(1));
  STAMP(9);
  {
    k.lane = fresh(k.lane); const int lane = k.lane, n16 = lane & 15, q4 = lane >> 4; const unsigned vL = lane * 16;
    const unsigned vC = fresh(yoff(crow0 + (lane & 7), 16 * w + (lane >> 3)));
    v4u y2[4], da[2][2][2], uu[2][2], wrc[2];
    auto ld_small = [&]() { sfor<4>([&](auto I) { constexpr int i = I; const unsigned e = (128 * w + 8 * (lane >> 3) + 64 * (i >> 1)) * 4 + (i & 1) * 16;
      da[0][i >> 1][i & 1] = ld128(k.rS, e, DA_OFF); da[1][i >> 1][i & 1] = ld128(k.rS, e, DA_OFF + 2048); uu[i >> 1][i & 1] = ld128(k.rS, e, U1_OFF); }); };
    ld_small();
    sfor<2>([&](auto I) { wrc[I] = __builtin_amdgcn_raw_buffer_load_b128(rWr, (128 * w + 8 * (lane >> 3) + 64 * I) * 2, 0, 0); });
    sfor<4>([&](auto J) { y2[J] = ld128(k.rS, vC, Y_OFF + Y_SZ + 2048 + J * YP); });
    const float m1 = lds_ld<float>(L_ST + (lane & 7) * 4), rs1 = lds_ld<float>(L_ST + 32 + (lane & 7) * 4);
    float x[16];
    sfor<2>([&](auto I) { constexpr int i = I; const v4u x1 = comb4(ring[4 * i], ring[4 * i + 1], ring[4 * i + 2], ring[4 * i + 3]);
#pragma unroll
      for (int e = 0; e < 8; ++e) { const float v = (e & 1) ? bhi(x1[e >> 1]) : blo(x1[e >> 1]); x[8 * i + e] = v + (v - m1) * rs1; } });
    auto bad_small = [&]() { unsigned m = 0; sfor<4>([&](auto I) { constexpr int i = I; m = maxv(maxv(maxv(m, da[0][i >> 1][i & 1]), da[1][i >> 1][i & 1]), uu[i >> 1][i & 1]); }); return anyl(m == POISON); };
    RETRY_WHILE(bad_small(), ld_small());
    const float w0 = blo(wcol0), w1 = blo(wcol1), wu_ = (unsigned)(crow0 + (lane & 7)) == s1.row ? 1.0f : 0.f;
#pragma unroll
    for (int e = 0; e < 16; ++e) x[e] += w0 * asf(da[0][e >> 3][(e >> 2) & 1][e & 3]) + w1 * asf(da[1][e >> 3][(e >> 2) & 1][e & 3]) + (wu_ != 0.f ? asf(uu[e >> 3][(e >> 2) & 1][e & 3]) : 0.f);
#pragma unroll
    for (int e = 0; e < 8; ++e) { const int i = e >> 1; x[e] += (e & 1) ? (bhi(ring[8][i]) + bhi(ring[9][i])) + (bhi(ring[10][i]) + bhi(ring[11][i])) : (blo(ring[8][i]) + blo(ring[9][i])) + (blo(ring[10][i]) + blo(ring[11][i])); }
    RETRY_WHILE(anyl(maxv(maxv(maxv(maxv(0u, y2[0]), y2[1]), y2[2]), y2[3]) == POISON), (sfor<4>([&](auto J) { y2[J] = ld128(k.rS, vC, Y_OFF + Y_SZ + 2048 + J * YP); })));
    STAMP(10);
#pragma unroll
    for (int e = 0; e < 8; ++e) { const int i = e >> 1; x[8 + e] += (e & 1) ? (bhi(y2[0][i]) + bhi(y2[1][i])) + (bhi(y2[2][i]) + bhi(y2[3][i])) : (blo(y2[0][i]) + blo(y2[1][i])) + (blo(y2[2][i]) + blo(y2[3][i])); }
    v4u x3[2]; float s = 0.f, qq = 0.f, dt = 0.f;
#pragma unroll
    for (int i = 0; i < 8; ++i) { const unsigned d = san(cvtpk(x[2 * i], x[2 * i + 1])); x3[i >> 2][i & 3] = d; x[2 * i] = blo(d); x[2 * i + 1] = bhi(d);
      s += x[2 * i] + x[2 * i + 1]; qq += x[2 * i] * x[2 * i] + x[2 * i + 1] * x[2 * i + 1]; dt += x[2 * i] * blo(wrc[i >> 2][i & 3]) + x[2 * i + 1] * bhi(wrc[i >> 2][i & 3]); }
    s = sum32(sum16(s + dpp<0x128>(s))); qq = sum32(sum16(qq + dpp<0x128>(qq))); dt = sum32(sum16(dt + dpp<0x128>(dt)));
    if (lane < 8) lds_st<f32x4>(L_CS + (lane * 4 + w) * 16, f32x4{s, qq, dt, 0.f});
    BAR();
    f32x4 st = lds_ld<f32x4>(L_CS + (lane & 7) * 64);
    sfor<3>([&](auto W) { st += lds_ld<f32x4>(L_CS + (lane & 7) * 64 + (W + 1) * 16); });
    const float m3 = st[0] * (1.0f / D_), rs3 = __builtin_amdgcn_rsqf(fmaxf(st[1] * (1.0f / D_) - m3 * m3, 0.f) + EPS), rv = rs3 * (st[2] - m3 * swr);
    float mx = fmaxf(rv, dpp<0xB1>(rv)); mx = fmaxf(mx, dpp<0x4E>(mx)); mx = fmaxf(mx, dpp<0x141>(mx));
    const unsigned bal = (unsigned)__ballot(rv == mx) & 0xffu;
    const int best = bal ? __builtin_ctz(bal) : 0;
    if (w == 0 && lane == best) st128(v4u{san(asu(rv)), san(asu(m3)), san(asu(rs3)), (unsigned)(crow0 + best)}, k.rS, k.b * 16, R2_OFF);
    if ((lane & 7) == best) sfor<2>([&](auto I) { st128(x3[I], k.rS, k.b * 1024 + (128 * w + 8 * (lane >> 3) + 64 * I) * 2, XR_OFF); });
    v4u rc[4];
    rec_load(k, rc, R2_OFF);
    v4u o[2];
#pragma unroll
    for (int i = 0; i < 8; ++i) o[i >> 2][i & 3] = cvtpk(x[2 * i] + (x[2 * i] - m3) * rs3, x[2 * i + 1] + (x[2 * i + 1] - m3) * rs3);
    const rsrc_t rO = mkrsrc(P.out, S_ * D_ * 2);
    const unsigned vO = (crow0 + (lane & 7)) * 1024 + (128 * w + 8 * (lane >> 3)) * 2;
    STAMP(11);
    const Sel s2 = rec_argmax(k, rc, R2_OFF);
    STAMP(12);
    unsigned xp = ld32(k.rS, s2.cu * 1024 + w * 256 + lane * 4, XR_OFF);
    unsigned xd = ld32(k.rS, dc >= 0 ? s2.cu * 1024 + dc * 16 + (lane & 6) * 2 : OOB, XR_OFF);
    if ((unsigned)(crow0 + (lane & 7)) != s2.row) sfor<2>([&](auto I) { __builtin_amdgcn_raw_buffer_store_b128(o[I], rO, vO + I * 128, 0, 0); });
    RETRY_WHILE(anyl(xp == POISON), (xp = ld32(k.rS, s2.cu * 1024 + w * 256 + lane * 4, XR_OFF)));
    ffn_h_partial(k, wgu, cvtpk((blo(xp) - s2.m) * s2.rs, (bhi(xp) - s2.m) * s2.rs));
    BAR();
    if (w == 0) ffn_h_publish(k, H2_OFF, fc);
    STAMP(13);
    if (dc >= 0) {
      unsigned hp[4];
      ffn_h_load(k, hp, H2_OFF);
      ffn_d_partial(k, wdn, hp, H2_OFF);
      BAR();
      if (w == 0) {
        RETRY_WHILE(anyl(xd == POISON), (xd = ld32(k.rS, s2.cu * 1024 + dc * 16 + (lane & 6) * 2, XR_OFF)));
        const float xv = (lane & 1) ? bhi(xd) : blo(xd);
        const float y = xv + (xv - s2.m) * s2.rs + ffn_d_sum(k) * sigmoidf(s2.r);
        const unsigned d = cvtpk(y, dpp<0xB1>(y));
        if (lane < 8 && !(lane & 1)) __builtin_amdgcn_raw_buffer_store_b32(d, rO, s2.row * 1024 + dc * 16 + lane * 2, 0, 0);
      }
    }
    if (k.b == 0 && w == 0 && lane == 0) st64(flag_val(k, 0), k.rS, 0, 0);
  }
  STAMP(14);
}
}

extern "C" void kernel_launch(void* const* d_in, const int* in_sizes, int n_in, void* d_out, int out_size, void* d_ws, size_t ws_size, hipStream_t stream) {
  Params P;
  P.x0 = (const u16*)d_in[0]; P.wsr = (const u16*)d_in[1]; P.wsi = (const u16*)d_in[2]; P.wdr = (const u16*)d_in[3]; P.wdi = (const u16*)d_in[4];
  P.wr = (const u16*)d_in[6]; P.wg = (const u16*)d_in[7]; P.wu = (const u16*)d_in[8]; P.wdn = (const u16*)d_in[9];
  P.out = (u16*)d_out; P.ws = (char*)d_ws;
  (void)hipFuncSetAttribute((const void*)fnet_mod_kernel, hipFuncAttributeMaxDynamicSharedMemorySize, LDS_BYTES);
  fnet_mod_kernel<<<256, 256, LDS_BYTES, stream>>>(P);
}
```

```cpp
#include <hip/hip_runtime.h>
#include <hip/hip_bf16.h>
#include <utility>

typedef unsigned v4u __attribute__((ext_vector_type(4)));
typedef unsigned v2u __attribute__((ext_vector_type(2)));
typedef float f32x4 __attribute__((ext_vector_type(4)));
typedef float f32x2 __attribute__((ext_vector_type(2)));
typedef __bf16 bf16x2 __attribute__((ext_vector_type(2)));
typedef unsigned short u16;
typedef __amdgpu_buffer_rsrc_t rsrc_t;
#define DI __device__ __forceinline__
#define LAS __attribute__((address_space(3)))
#define SB() __builtin_amdgcn_sched_barrier(0)
#define BAR() asm volatile("s_waitcnt lgkmcnt(0)\n\ts_barrier" ::: "memory")
#define IC(v) std::integral_constant<int, (v)>{}

#define X_NT 0
#define WD_NT 0
#define WS_NT 0
#define WS_ROT 1
#define DLY_R2 6
#define DLY_AB 4
#define DLY_PY 12
#define OUT_AUX 0
#define RH_MODE 1
#define GG 10
#define PK0 11
#define T_RL 22
#define T_PL (2 * 64 + 25)
#define T_PR (3 * 64 + 9)
#define T_PU (3 * 64 + 23)
#define T_UL (4 * 64 + 58)
#define T_UM (5 * 64 + 42)
#define T_UF (5 * 64 + 58)
#define CODE_TOUCH 6
#define WS_TOUCH 1
#define RETOUCH 2
#define G_NT 0
#define GAP_R1 5
#define GAP_C 3
#define GAP_R2 4
#define GAP_PY 4
#define DLY_C 8
#define AB_GATE 1
#define R1POS 1
#define RN2 16
#define RN1 16
#define DIAG 0
#define STAMP(i) do {} while (0)
#define STAMPC(i) do {} while (0)

namespace {
constexpr int S_ = 2048, D_ = 512, F_ = 1792;
constexpr float EPS = 1e-5f;
constexpr unsigned FLAG_OFF = 26u << 20, FSTEP = 131072;
constexpr unsigned SM_OFF = 262144;
constexpr unsigned R1_OFF = SM_OFF, R2_OFF = SM_OFF + 16384, U1_OFF = SM_OFF + 49152,
                   DA_OFF = SM_OFF + 65536, XR_OFF = SM_OFF + 98304, PY_OFF = 1u << 20, PY_SZ = 512u << 10;
constexpr unsigned AB_OFF = 2u << 20, AB_SZ = 4u << 20;
constexpr unsigned Y_OFF = 10u << 20, Y_SZ = 8u << 20, YP = 2u << 20;
constexpr unsigned WS_BYTES = 28u << 20, DBG_OFF = (27u << 20) + 131072;
constexpr unsigned OOB = 0x7f000000u, POISON = 0xffffffffu;
constexpr int SC1 = 16;
constexpr int SPIN_CAP = 1 << 16;
constexpr unsigned L_RED = 131072, L_MISC = 155648, L_WR = L_MISC, L_ST = L_MISC + 1024, L_FH = L_MISC + 1152, L_FHC = L_MISC + 1408, L_FD = L_MISC + 1536,
                   L_CSUM = L_MISC + 1664, L_CS = L_MISC + 2176, L_XB = L_CS, L_FW = L_MISC + 3200, LDS_BYTES = 163840;

template <class F, int... I> DI void sfor_(F&& f, std::integer_sequence<int, I...>) { (f(std::integral_constant<int, I>{}), ...); }
template <int N, class F> DI void sfor(F&& f) { sfor_(f, std::make_integer_sequence<int, N>{}); }
DI unsigned rfl(unsigned x) { return __builtin_amdgcn_readfirstlane(x); }
DI float asf(unsigned x) { return __builtin_bit_cast(float, x); }
DI unsigned asu(float x) { return __builtin_bit_cast(unsigned, x); }
DI float blo(unsigned d) { return asf(d << 16); }
DI float bhi(unsigned d) { return asf(d & 0xffff0000u); }
DI unsigned cvtpk(float lo, float hi) { f32x2 v = {lo, hi}; bf16x2 b = __builtin_convertvector(v, bf16x2); return __builtin_bit_cast(unsigned, b); }
DI unsigned san(unsigned d) { return min(d, 0xfffffffeu); }
DI float fmx(float a, float b) { float r; asm volatile("v_max_f32 %0, %1, %2" : "=v"(r) : "v"(a), "v"(b)); return r; }
DI float pinf(float a) { asm volatile("" : "+v"(a)); return a; }
DI unsigned pinu(unsigned a) { asm volatile("" : "+v"(a)); return a; }
DI float opq(float a) { asm("" : "+v"(a)); return a; }
DI unsigned max3u(unsigned a, unsigned b, unsigned c) { return max(max(a, b), c); }
DI unsigned maxv(unsigned m, const v4u& v) { return max3u(max3u(m, v[0], v[1]), v[2], v[3]); }
DI float dot2(unsigned a, unsigned b, float c) { float r; asm("v_dot2_f32_bf16 %0, %1, %2, %3" : "=v"(r) : "v"(a), "v"(b), "v"(c)); return r; }
DI float dot2z(unsigned a, unsigned b) { float r; asm("v_dot2_f32_bf16 %0, %1, %2, 0" : "=v"(r) : "v"(a), "v"(b)); return r; }
DI unsigned plo(unsigned a, unsigned b) { return __builtin_amdgcn_perm(b, a, 0x05040100u); }
DI unsigned phi(unsigned a, unsigned b) { return __builtin_amdgcn_perm(b, a, 0x07060302u); }
template <int C> DI float dpp(float v) { return asf(__builtin_amdgcn_update_dpp(0, asu(v), C, 0xf, 0xf, true)); }
DI float sum16(float v) { auto r = __builtin_amdgcn_permlane16_swap(asu(v), asu(v), false, false); return asf(r[0]) + asf(r[1]); }
DI float sum32(float v) { auto r = __builtin_amdgcn_permlane32_swap(asu(v), asu(v), false, false); return asf(r[0]) + asf(r[1]); }
DI float max16(float v) { auto r = __builtin_amdgcn_permlane16_swap(asu(v), asu(v), false, false); return fmx(asf(r[0]), asf(r[1])); }
DI float max32(float v) { auto r = __builtin_amdgcn_permlane32_swap(asu(v), asu(v), false, false); return fmx(asf(r[0]), asf(r[1])); }
DI float rowsum(float v) { v += dpp<0x128>(v); v += dpp<0x124>(v); v += dpp<0x122>(v); v += dpp<0x121>(v); return v; }
DI float wavesum(float v) { return sum32(sum16(rowsum(v))); }
DI float wavemax(float v) { v = fmx(v, dpp<0x128>(v)); v = fmx(v, dpp<0x124>(v)); v = fmx(v, dpp<0x122>(v)); v = fmx(v, dpp<0x121>(v)); return max32(max16(v)); }
DI float fold32(float a, float b) { auto r = __builtin_amdgcn_permlane32_swap(asu(a), asu(b), false, false); return asf(r[0]) + asf(r[1]); }
DI float fold16(float a, float b) { auto r = __builtin_amdgcn_permlane16_swap(asu(a), asu(b), false, false); return asf(r[0]) + asf(r[1]); }
DI float sigmoidf(float x) { return __builtin_amdgcn_rcpf(1.0f + __builtin_amdgcn_exp2f(-1.44269504089f * x)); }

DI void mfa0(f32x4& d, const v4u& a, const v4u& b) { asm volatile("v_mfma_f32_16x16x32_bf16 %0, %1, %2, 0" : "=&v"(d) : "v"(a), "a"(b)); }
DI void mfa1(f32x4& d, const v4u& a, const v4u& b) { asm volatile("v_mfma_f32_16x16x32_bf16 %0, %1, %2, %0" : "+v"(d) : "v"(a), "a"(b)); }
DI void mfac(f32x4& d, const v4u& a, const v4u& b, const f32x4& c) { asm volatile("v_mfma_f32_16x16x32_bf16 %0, %1, %2, %3" : "=&v"(d) : "v"(a), "a"(b), "v"(c)); }
DI void mfv0(f32x4& d, const v4u& a, const v4u& b) { asm volatile("v_mfma_f32_16x16x32_bf16 %0, %1, %2, 0" : "=&v"(d) : "v"(a), "v"(b)); }
DI void mfv1(f32x4& d, const v4u& a, const v4u& b) { asm volatile("v_mfma_f32_16x16x32_bf16 %0, %1, %2, %0" : "+v"(d) : "v"(a), "v"(b)); }
template <bool FIRST> DI void mfv(f32x4& d, const v4u& a, const v4u& b) { if constexpr (FIRST) mfv0(d, a, b); else mfv1(d, a, b); }
DI v4u splat(unsigned c) { v4u v = {c, c, c, c}; asm volatile("s_nop 1" : "+v"(v)); return v; }
DI void nop2() { asm volatile("s_nop 1"); }
DI void nop8() { asm volatile("s_nop 7"); }
DI void set_m0(unsigned base, int add) { asm volatile("s_add_u32 m0, %0, %1" :: "s"(base), "i"(add) : "scc", "memory"); }
template <bool NT> DI void dma_go(unsigned voff, rsrc_t r, unsigned so) {
  if constexpr (NT) asm volatile("buffer_load_dwordx4 %0, %1, %2 offen nt lds" :: "v"(voff), "s"(r), "s"(so) : "memory");
  else asm volatile("buffer_load_dwordx4 %0, %1, %2 offen lds" :: "v"(voff), "s"(r), "s"(so) : "memory"); }
DI void glds32(unsigned m0, const void* p) { asm volatile("s_mov_b32 m0, %0\n\ts_nop 0\n\tglobal_load_lds_dword %1, off" :: "s"(m0), "v"(p) : "memory"); }
DI void touch(unsigned m0, unsigned voff, rsrc_t r) {
  asm volatile("s_mov_b32 m0, %0\n\ts_nop 0\n\tbuffer_load_dword %1, %2, 0 offen lds" :: "s"(m0), "v"(voff), "s"(r) : "memory"); }
DI void dma(unsigned m0, unsigned voff, rsrc_t r, unsigned so) {
  asm volatile("s_mov_b32 m0, %0\n\ts_nop 0\n\tbuffer_load_dwordx4 %1, %2, %3 offen lds" :: "s"(m0), "v"(voff), "s"(r), "s"(so) : "memory"); }
DI void dma_nt(unsigned m0, unsigned voff, rsrc_t r, unsigned so) {
  asm volatile("s_mov_b32 m0, %0\n\ts_nop 0\n\tbuffer_load_dwordx4 %1, %2, %3 offen nt lds" :: "s"(m0), "v"(voff), "s"(r), "s"(so) : "memory"); }
template <bool NT> DI void dma_x(unsigned m0, unsigned voff, rsrc_t r, unsigned so) { if constexpr (NT) dma_nt(m0, voff, r, so); else dma(m0, voff, r, so); }
template <bool NT> DI void ld128_a(v4u& d, unsigned voff, rsrc_t r, unsigned so) {
  if constexpr (NT) asm volatile("buffer_load_dwordx4 %0, %1, %2, %3 offen nt" : "=a"(d) : "v"(voff), "s"(r), "s"(so) : "memory");
  else asm volatile("buffer_load_dwordx4 %0, %1, %2, %3 offen" : "=a"(d) : "v"(voff), "s"(r), "s"(so) : "memory"); }
template <int OFF> DI void turn_a(v4u& p, v4u& q, unsigned wr, unsigned r0, unsigned r1) {
  asm volatile("ds_write_b128 %2, %0 offset:%5\n\tds_write_b128 %2, %1 offset:%6\n\tds_read_b128 %0, %3 offset:%5\n\tds_read_b128 %1, %4 offset:%5"
               : "+a"(p), "+a"(q) : "v"(wr), "v"(r0), "v"(r1), "i"(OFF), "i"(OFF + 1024) : "memory"); }
template <int OFF> DI void dsr_a(v4u& d, unsigned addr) { asm volatile("ds_read_b128 %0, %1 offset:%2" : "=a"(d) : "v"(addr), "i"(OFF) : "memory"); }
template <int N> DI void waitvm() { asm volatile("s_waitcnt vmcnt(%0)" :: "i"(N) : "memory"); }
DI void waitlgkm() { asm volatile("s_waitcnt lgkmcnt(0)" ::: "memory"); }
DI void st128_asm(const v4u& v, unsigned voff, rsrc_t r, unsigned so) {
  asm volatile("buffer_store_dwordx4 %0, %1, %2, %3 offen sc1\n\ts_nop 1" :: "v"(v), "v"(voff), "s"(r), "s"(so) : "memory"); }
DI void st64_asm(const v2u& v, unsigned voff, rsrc_t r) { asm volatile("buffer_store_dwordx2 %0, %1, %2, 0 offen sc1" :: "v"(v), "v"(voff), "s"(r) : "memory"); }
DI void ld64_asm(v2u& v, unsigned voff, rsrc_t r) { asm volatile("buffer_load_dwordx2 %0, %1, %2, 0 offen sc1" : "=v"(v) : "v"(voff), "s"(r) : "memory"); }
DI void ld128_asm(v4u& v, unsigned voff, rsrc_t r) { asm volatile("buffer_load_dwordx4 %0, %1, %2, 0 offen" : "=v"(v) : "v"(voff), "s"(r) : "memory"); }
DI void pin(v2u& v) { asm volatile("" : "+v"(v)); }
DI void pinp(const void*& p) { asm volatile("" : "+v"(p)); }
DI void pin(f32x4& v) { asm volatile("" : "+v"(v)); }
DI void pin(v4u& v) { asm volatile("" : "+v"(v)); }
DI v4u ld128(rsrc_t r, unsigned v, unsigned s) { return __builtin_amdgcn_raw_buffer_load_b128(r, v, s, SC1); }
DI v2u ld64(rsrc_t r, unsigned v, unsigned s) { return __builtin_amdgcn_raw_buffer_load_b64(r, v, s, SC1); }
DI unsigned ld32(rsrc_t r, unsigned v, unsigned s) { return __builtin_amdgcn_raw_buffer_load_b32(r, v, s, SC1); }
DI void st128(const v4u& d, rsrc_t r, unsigned v, unsigned s) { __builtin_amdgcn_raw_buffer_store_b128(d, r, v, s, SC1); }
DI void st64(const v2u& d, rsrc_t r, unsigned v, unsigned s) { __builtin_amdgcn_raw_buffer_store_b64(d, r, v, s, SC1); }
DI void st32(unsigned d, rsrc_t r, unsigned v, unsigned s) { __builtin_amdgcn_raw_buffer_store_b32(d, r, v, s, SC1); }
template <class T> DI T lds_ld(unsigned a) { return *(LAS T*)(size_t)a; }
template <class T> DI void lds_st(unsigned a, const T& v) { *(LAS T*)(size_t)a = v; }
DI void fence_c() { asm volatile("" ::: "memory"); }
DI rsrc_t mkrsrc(const void* p, unsigned bytes) { return __builtin_amdgcn_make_buffer_rsrc((void*)p, 0, bytes, 0x00020000); }

DI int fresh(int v) { asm volatile("" : "+v"(v)); return v; }
DI unsigned frag_addr(int lane, int par) { const int n = lane & 15, q = lane >> 4; return 128 * n + 16 * ((4 * par + q) ^ (n >> 1)); }
struct Ctx {
  rsrc_t rS;
  int lane, w, b, c, kh, tn, gA, gB, rh, sl2, xc;
  unsigned long long T;
};
DI bool anyl(bool p) { return __builtin_amdgcn_ballot_w64(p) != 0ull; }
#define RETRY_WHILE(bad, reload) do { if (__builtin_expect((bad), 0)) { int spin_ = 0; _Pragma("unroll 1") do { fence_c(); reload; } while ((bad) && ++spin_ < SPIN_CAP); } fence_c(); } while (0)
#define RETRY2_WHILE(x, y, bad, load, POLL_GAP) do { if (__builtin_expect(bad(x), 0)) { load(x); int spin_ = 0; _Pragma("unroll 1") do { \
    __builtin_amdgcn_s_sleep(POLL_GAP); fence_c(); load(y); SB(); if (!bad(x)) break; \
    __builtin_amdgcn_s_sleep(POLL_GAP); fence_c(); load(x); SB(); if (!bad(y) || ++spin_ >= SPIN_CAP) { x = y; break; } } while (true); } fence_c(); } while (0)
struct V4 { v4u v[4]; };
struct V2 { v4u v[2]; };
DI float fdot(unsigned a, unsigned b, float c) { return __builtin_amdgcn_fdot2_f32_bf16(__builtin_bit_cast(bf16x2, a), __builtin_bit_cast(bf16x2, b), c, false); }
DI bool flag_bad(const Ctx& k, const v2u& f) { return anyl(__builtin_bit_cast(unsigned long long, f) != k.T); }
DI v2u flag_ld(const Ctx& k, unsigned voff, int step, unsigned so = 0) { return ld64(k.rS, voff, FLAG_OFF + step * FSTEP + so); }
DI void flag_wait(const Ctx& k, v2u f, unsigned voff, int step) { RETRY_WHILE(flag_bad(k, f), f = flag_ld(k, voff, step)); }
DI void flag_set(const Ctx& k, unsigned voff, int step) { st64(__builtin_bit_cast(v2u, k.T), k.rS, voff, FLAG_OFF + step * FSTEP); }
DI unsigned blk_of_A(int c, int tn, int gA) { return (unsigned)(((((gA & 7) << 2) | tn) << 3) | ((gA >> 3) << 1) | c); }
DI unsigned blk_of_B(int c, int kh, int tn, int gB) { return (unsigned)(((((gB & 7) << 2) | tn) << 3) | ((gB >> 3) << 2) | (kh << 1) | c); }

DI void add4(float* x, const v4u& a, const v4u& b, const v4u& c, const v4u& d, unsigned lo, unsigned hi) {
#pragma unroll
  for (int e = 0; e < 8; ++e) { const unsigned sl = (e & 1) ? hi : lo; x[e] = fdot(a[e >> 1], sl, fdot(b[e >> 1], sl, fdot(c[e >> 1], sl, fdot(d[e >> 1], sl, x[e])))); }
}
DI v4u comb4(const v4u& a, const v4u& b, const v4u& c, const v4u& d) {
  v4u o;
#pragma unroll
  for (int i = 0; i < 4; ++i) o[i] = cvtpk((blo(a[i]) + blo(b[i])) + (blo(c[i]) + blo(d[i])), (bhi(a[i]) + bhi(b[i])) + (bhi(c[i]) + bhi(d[i])));
  return o;
}

DI float a_diag(const f32x4& S2) {
  constexpr unsigned long long M = 0x1000010000100001ull;
  float t;
  asm volatile("v_cndmask_b32 %0, 0, %1, %5\n\tv_cndmask_b32 %0, %0, %2, %6\n\tv_cndmask_b32 %0, %0, %3, %7\n\tv_cndmask_b32 %0, %0, %4, %8"
               : "=&v"(t) : "v"(S2[0]), "v"(S2[1]), "v"(S2[2]), "v"(S2[3]), "s"(M), "s"(M << 1), "s"(M << 2), "s"(M << 3));
  return t;
}
DI void a_spread(f32x4& Q, float t, float one) { asm volatile("s_nop 1\n\tv_mfma_f32_16x16x4_f32 %0, %1, %2, 0" : "=&v"(Q) : "v"(t), "v"(one)); }
template <int LAYER> DI void a_scale(const Ctx& k, const f32x4& Q, const f32x4& mean, f32x4& vari, f32x4& rstd, f32x4& sc) {
#pragma unroll
  for (int r = 0; r < 4; ++r) {
    const float var = fmaxf(Q[r] * (1.0f / D_) - mean[r] * mean[r], 0.f), rs = __builtin_amdgcn_rsqf(var + EPS);
    vari[r] = var; rstd[r] = rs;
    float s = rs;
    if constexpr (LAYER == 1) { const float g = 1.0f + rs; s = g * __builtin_amdgcn_rsqf(var * g * g + EPS); }
    sc[r] = k.c ? -s : s;
  }
}
template <int LAYER> DI void a_stats(const Ctx& k, const f32x4& S1, const f32x4& S2, f32x4& mean, f32x4& vari, f32x4& rstd, f32x4& sc) {
  f32x4 Q; a_spread(Q, a_diag(S2), 1.0f);
  mean = S1 * (1.0f / D_); pin(mean);
  asm volatile("s_nop 7\n\ts_nop 1" : "+v"(Q));
  a_scale<LAYER>(k, Q, mean, vari, rstd, sc);
}
template <int LAYER, int I> DI void a_emit1(const Ctx& k, const f32x4 (&U)[8], const float (&cs)[8], const f32x4& mean, const f32x4& sc) {
  const int e4 = k.lane & 15, rho = k.lane >> 4;
  const unsigned voff = (unsigned)((rho & 1) * 65536 + (2 * k.gA + (k.w >> 1)) * 1024 + ((2 * (k.w & 1) + (rho >> 1)) * 16 + e4) * 16);
  const unsigned so = AB_OFF + LAYER * AB_SZ + (k.c * 4 + k.tn) * 524288 + I * 131072;
  unsigned pk[4];
#pragma unroll
  for (int h = 0; h < 2; ++h) {
    const f32x4 v = (U[2 * I + h] - mean * cs[2 * I + h]) * sc;
    pk[2 * h] = cvtpk(v[0], v[1]); pk[2 * h + 1] = cvtpk(v[2], v[3]);
    if constexpr (LAYER == 1 && I < 2) { pk[2 * h] = san(pk[2 * h]); pk[2 * h + 1] = san(pk[2 * h + 1]); }
  }
  auto e0 = __builtin_amdgcn_permlane16_swap(pk[0], pk[2], false, false), e1 = __builtin_amdgcn_permlane16_swap(pk[1], pk[3], false, false);
  const v4u o = {e0[0], e1[0], e0[1], e1[1]};
  if constexpr (LAYER == 0) st128_asm(o, voff, k.rS, so); else st128(o, k.rS, voff, so);
  SB();
}

struct Sel { float r, m, var, rs; unsigned row, cu; };
DI int best_lane(float r) { const unsigned long long bal = __builtin_amdgcn_ballot_w64(r == wavemax(r)); return bal ? __builtin_ctzll(bal) : 0; }
DI Sel sel_of(const v4u& rec, unsigned cu, int L) {
  Sel s;
  s.r = asf(__builtin_amdgcn_readlane(rec[0], L)); s.m = asf(__builtin_amdgcn_readlane(rec[1], L)); s.var = asf(__builtin_amdgcn_readlane(rec[2], L));
  s.row = __builtin_amdgcn_readlane(rec[3], L) & (S_ - 1); s.cu = cu;
  s.rs = __builtin_amdgcn_rsqf(s.var + EPS);
  return s;
}
DI void rec_store(const Ctx& k, const v4u& rec, int L, bool on, unsigned idx, unsigned off, unsigned copy) {
  v4u b; sfor<4>([&](auto I) { b[(int)I] = __builtin_amdgcn_readlane(rec[(int)I], L); });
  st128(b, k.rS, (on && k.lane < 8) ? k.lane * copy + idx * 16 : OOB, off);
}
DI unsigned tr_addr(unsigned l, unsigned slot) { return slot + ((l >> 1) & 1) * 512 + (8 * (l >> 4) + ((l & 15) >> 2)) * 16 + (l & 1) * 8; }
DI v2u tr_read(unsigned a) { typedef short v4s __attribute__((ext_vector_type(4))); return __builtin_bit_cast(v2u, __builtin_amdgcn_ds_read_tr16_b64_v4i16((LAS v4s*)(size_t)a)); }
DI v4u to_frag(const Ctx& k, const v4u& raw, unsigned slot) {
  lds_st<v4u>(slot + k.lane * 16, raw);
  const unsigned a = tr_addr(k.lane, slot);
  const v2u lo = tr_read(a), hi = tr_read(a + 64);
  return v4u{lo[0], lo[1], hi[0], hi[1]};
}
template <int N> DI f32x4 gemv(const v4u (&wf)[N], const v4u (&x)[N]) {
  f32x4 d; SB(); nop2();
  sfor<N>([&](auto I) { mfv<I == 0>(d, wf[I], x[I]); });
  nop8(); SB();
  return d;
}
DI void ffn_down8(const Ctx& k, unsigned d, const unsigned (&wd)[8], unsigned voff, unsigned soff) {
  float y0 = 0.f, y1 = 0.f;
  sfor<4>([&](auto I) { const bf16x2 hp = __builtin_bit_cast(bf16x2, __builtin_amdgcn_readlane(d, 2 * I));
    y0 = __builtin_amdgcn_fdot2_f32_bf16(__builtin_bit_cast(bf16x2, wd[2 * I]), hp, y0, false); y1 = __builtin_amdgcn_fdot2_f32_bf16(__builtin_bit_cast(bf16x2, wd[2 * I + 1]), hp, y1, false); });
  st64(v2u{san(asu(y0)), san(asu(y1))}, k.rS, voff, soff);
}
DI void ffn_mid(const Ctx& k, const Sel& s, const unsigned (&wd)[8], unsigned off, int fc) {
  const unsigned a = (k.lane & 7) * 4; float g = 0.f, u = 0.f;
  sfor<4>([&](auto W) { g += lds_ld<float>(L_FH + W * 64 + a); u += lds_ld<float>(L_FH + W * 64 + 32 + a); });
  g *= s.rs; u *= s.rs;
  const float h = g * sigmoidf(g) * u;
  ffn_down8(k, cvtpk(h, dpp<0xB1>(h)), wd, fc >= 0 ? k.w * 512 + k.lane * 8 : OOB, off + fc * 2048);
}
DI void ffn_red_load(const Ctx& k, v4u (&py)[2], unsigned off, int dc) {
  sfor<2>([&](auto I) { py[I] = ld128(k.rS, k.lane < 56 ? (56 * k.w + k.lane) * 2048 + I * 16 : OOB, off + dc * 32); });
}
DI void ffn_red_partial(const Ctx& k, v4u (&py)[2], unsigned off, int dc) {
  RETRY_WHILE(anyl(maxv(maxv(0u, py[0]), py[1]) == POISON), ffn_red_load(k, py, off, dc));
  float t[4], u[2];
  sfor<4>([&](auto I) { t[I] = fold32(asf(py[I >> 1][2 * (I & 1)]), asf(py[I >> 1][2 * (I & 1) + 1])); });
  sfor<2>([&](auto I) { u[I] = rowsum(fold16(t[2 * I], t[2 * I + 1])); });
  if ((k.lane & 15) == 0) lds_st<f32x2>(L_FD + k.w * 32 + (2 * ((k.lane >> 4) & 1) + (k.lane >> 5)) * 8, f32x2{u[0], u[1]});
}
DI float ffn_d_sum(const Ctx& k) {
  const int f = k.lane & 7; float y = 0.f;
  sfor<4>([&](auto W) { y += lds_ld<float>(L_FD + W * 32 + ((f & 3) * 2 + (f >> 2)) * 4); });
  return y;
}
DI float sum8(float v) { v += dpp<0xB1>(v); v += dpp<0x4E>(v); v += dpp<0x141>(v); return v; }
DI unsigned comb1(const unsigned (&p)[4]) { return cvtpk((blo(p[0]) + blo(p[1])) + (blo(p[2]) + blo(p[3])), (bhi(p[0]) + bhi(p[1])) + (bhi(p[2]) + bhi(p[3]))); }
DI unsigned yoff(unsigned row, unsigned ch) { return (((row >> 4) * 16 + (ch >> 2)) * 64 + (ch & 3) * 16 + (row & 15)) * 16; }

struct Params { const u16 *x0, *wsr, *wsi, *wdr, *wdi, *wr, *wg, *wu, *wdn; u16* out; char* ws; };

#define PHASE_LANE k.lane = fresh(k.lane); const int lane = k.lane, n16 = lane & 15, q4 = lane >> 4; const unsigned vL = lane * 16

__global__ void __launch_bounds__(256, 1) fnet_mod_kernel(Params P) {
  extern __shared__ __attribute__((aligned(16))) char smem[];
  STAMP(0);
  const char* code0; asm volatile("s_getpc_b64 %0" : "=s"(code0));
  Ctx k;
  k.b = blockIdx.x; k.lane = threadIdx.x & 63; k.w = rfl(threadIdx.x >> 6);
  k.xc = k.b & 7; k.sl2 = k.b >> 5; k.c = k.xc & 1; k.kh = (k.xc >> 1) & 1; k.tn = (k.b >> 3) & 3;
  k.gB = (k.xc >> 2) * 8 + k.sl2; k.gA = (k.xc >> 1) * 8 + k.sl2; k.rh = RH_MODE == 0 ? 0 : RH_MODE == 1 ? k.sl2 & 3 : RH_MODE == 2 ? (k.sl2 ^ k.tn) & 3 : RH_MODE == 3 ? ((k.sl2 ^ k.tn) & 1) * 2 : (k.sl2 & 1) * 2;
  k.rS = mkrsrc(P.ws, WS_BYTES);
  const int w = k.w;
  const rsrc_t rX = mkrsrc(P.x0, S_ * D_ * 2), rWr = mkrsrc(P.wr, D_ * 2), rWd = mkrsrc(k.c ? P.wdi : P.wdr, D_ * D_ * 2);
  const unsigned tx = (2 * w) ^ (WS_ROT ? k.tn : 0) ^ k.c;
  auto flagA = [&](int n16) { return (blk_of_A(k.c, k.tn, 16 * k.kh + 4 * w + (n16 >> 2)) * 4 + (n16 & 3)) * 128; };
  auto flagY = [&](int n16, int rw) { return (blk_of_B(n16 & 1, (n16 >> 1) & 1, n16 >> 2, k.gA >> 1) * 4 + (((4 * (k.gA & 1) + rw) ^ (WS_ROT ? n16 >> 2 : 0)) >> 1)) * 128; };
  auto flagMe = [&](int lane) { return lane == 0 ? (k.b * 4 + w) * 128 : OOB; };
  const int jj = k.c * 4 + k.tn, crow0 = 64 * k.gA + 8 * jj;
  const int mem = (k.sl2 & 1) * 4 + k.tn;
  const int fg = k.xc + 8 * (k.sl2 >> 1), fc = fg < 28 ? fg * 8 + mem : -1;
  const int dc = k.sl2 >= 6 ? k.xc * 8 + mem : -1;
  const int an = k.sl2 < 2 ? k.sl2 * 4 + (k.xc >> 1) : -1;

  v4u nx[16];
  unsigned dr[8]; v4u wgu[4], graw[8];
  float swr;
  v4u Wa[64];
  {
    PHASE_LANE;
    const rsrc_t rW = mkrsrc(k.c ? P.wsi : P.wsr, S_ * S_ * 2);
    v2u ep; ld64_asm(ep, 0u, k.rS);
    const unsigned ring = L_RED + w * 8192;
    unsigned vx[4], vw[4];
    const unsigned sw0 = ((lane & 7) ^ (lane >> 4)) * 16, sw1 = ((lane & 7) ^ (4 + (lane >> 4))) * 16;
    sfor<4>([&](auto I) { constexpr int i = I; vx[i] = (4 * i + q4) * 1024 + (n16 ^ (4 * i + q4)) * 16; vw[i] = (8 * i + (lane >> 3)) * 1024 + ((i & 1) ? sw1 : sw0); });
    const unsigned vs0 = (lane >> 3) * 4096 + sw0, vs1 = (8 + (lane >> 3)) * 4096 + sw1;
    const unsigned xlo = 98304 + w * 4096, wlds = w * 4096, sox = (64 * k.gA + 16 * w) * 1024, sow = (128 * k.tn + 32 * w) * 1024;
    unsigned qo[4]; sfor<4>([&](auto Q) { qo[Q] = (Q ^ k.rh) * 256; });
    const unsigned sb = (128 * k.gB) * 4096 + (1024 * k.kh + 256 * w) * 2;
    auto p_m0 = [&](auto N) { constexpr int n = N;
      if constexpr (n < 4 || (n >= 12 && n < 16)) set_m0(xlo, (n >= 12) * 16384 + (n & 3) * 1024);
      else if constexpr (n >= 24 && n < 32) set_m0(ring, (n - 24) * 1024);
      else { constexpr int q = n < 12 ? 0 : n < 24 ? 1 : n < 40 ? 2 : 3, i = (n < 12 ? n - 4 : n) & 7; set_m0(wlds, q * 32768 + (i >> 2) * 16384 + (i & 3) * 1024); } };
    auto p_go = [&](auto N) { constexpr int n = N;
      if constexpr (n < 4 || (n >= 12 && n < 16) || (n >= 24 && n < 32)) { constexpr int q = n < 4 ? 0 : n < 16 ? 1 : n < 28 ? 2 : 3; dma_go<X_NT>(vx[n & 3], rX, sox + qo[q]); }
      else { constexpr int q = n < 12 ? 0 : n < 24 ? 1 : n < 40 ? 2 : 3, i = (n < 12 ? n - 4 : n) & 7;
        dma_go<(WD_NT == 2 || (WD_NT == 1 && q == 0))>(vw[i & 3], rWd, sow + qo[q] + (i >> 2) * 128); } };
    auto wsl = [&](auto I) { constexpr int i = I; ld128_a<(WS_NT == 2 || (WS_NT == 1 && ((i >> 3) & 3) == 0))>(Wa[i], (i & 1) ? vs1 : vs0, rW, sb + ((i >> 3) ^ tx) * 65536 + ((i >> 1) & 3) * 128); };
    auto piece = [&](auto N) { p_m0(N); asm volatile("s_nop 0"); p_go(N); };
    auto pretouch = [&]() {
      const void* t = k.c ? P.wsr : P.wsi;
      auto put = [&](bool on, const void* p) { t = on ? p : t; pinp(t); };
      put(lane == 0, P.wg); put(lane == 1, P.wu); put(lane == 2, P.wdn); put(lane == 3, P.out);
      if (CODE_TOUCH) put(lane >= 8 && lane < 8 + CODE_TOUCH, code0 + (((k.b >> 3) * 4 + w) * CODE_TOUCH + lane - 8) * 64);
      if (WS_TOUCH) put(lane >= 32 && lane < 46, P.ws + (lane - 32) * (2u << 20) + 4096);
      glds32(65536u + w * 256u, t); };
    v4u xf[16], ones, wrv, wf[10];
    unsigned xb[4], fb[2][2], cb[2][2], rd0, rd1, wrr, vFA;
    f32x4 U[8], S1, S2, C0, C1, mean, vari, rstd, sc; float cs[8]; v2u fl;
    auto setup_rest = [&]() {
      sfor<4>([&](auto Pp) { xb[Pp] = n16 * 256 + ((4 * Pp + q4) ^ n16) * 16; });
      sfor<4>([&](auto I) { constexpr int i = I; fb[i >> 1][i & 1] = fresh(frag_addr(lane, i & 1) + (i >> 1) * 65536); cb[i >> 1][i & 1] = fresh(fb[i >> 1][i & 1] + w * 4096); });
      rd0 = fresh(ring + fb[0][0]); rd1 = fresh(ring + fb[0][1]); wrr = fresh(ring + vL); vFA = fresh(flagA(n16));
      ones = splat(0x3f803f80u); };
    auto rdx = [&](auto Q) { constexpr int q = Q; sfor<4>([&](auto Pp) { xf[4 * q + Pp] = lds_ld<v4u>((q < 2 ? xlo + q * 16384 : ring + (q - 2) * 4096) + xb[Pp]); }); };
    auto rdw = [&](auto I, auto N) { constexpr int i = I, nn = N, q = i >> 2, p = i & 3;
      if constexpr (nn < 8) wf[nn] = lds_ld<v4u>(fb[q >> 1][p & 1] + ((q & 1) * 32768 + (p >> 1) * 16384 + nn * 2048));
      else wf[nn] = lds_ld<v4u>(cb[q >> 1][p & 1] + ((q & 1) * 32768 + (p >> 1) * 16384 + (nn - 8) * 2048)); };
    auto after_first_wait = [&]() {
      pin(ep);
      k.T = (((unsigned long long)rfl(ep[1]) << 32) | rfl(ep[0])) + 1ull;
      rdx(IC(0));
      BAR();
      STAMP(1);
      sfor<10>([&](auto N) { rdw(IC(0), N); }); SB(); };
    auto a1_mfma = [&](auto I, auto M) { constexpr int i = I, m = M; constexpr bool first = i == 0;
      if constexpr (m == 0) mfv<first>(S1, xf[i], ones); else if constexpr (m == 1) mfv<first>(S2, xf[i], xf[i]);
      else if constexpr (m < 10) mfv<first>(U[m - 2], xf[i], wf[m - 2]); else if constexpr (m == 10) mfv<first>(C0, ones, wf[8]); else mfv<first>(C1, ones, wf[9]);
      SB(); };
    auto a1_refill = [&](auto I, auto M) { constexpr int i = I, m = M; if constexpr (m >= 2 && i < 15) rdw(IC(i + 1), IC(m - 2)); SB(); };
    auto pattern_and_small_loads = [&]() {
      const v4u pz = splat(POISON);
      st128_asm(pz, w == 0 ? XR_OFF + k.b * 1024 + vL : (w == 1 && lane < 24) ? SM_OFF + k.b * 384 + vL : OOB, k.rS, 0);
      st128_asm(pz, k.b * 4096 + w * 1024 + vL, k.rS, PY_OFF);
      ld128_asm(wrv, vL, rWr);
      sfor<8>([&](auto J) { dr[J] = __builtin_amdgcn_raw_buffer_load_b32(mkrsrc(P.wdn, D_ * F_ * 2), w * 256 + lane * 4, ((fc >= 0 ? fc : 0) * 8 + J) * (D_ * 2), G_NT ? 2 : 0); }); };
    auto exchange_cs = [&]() {
      nop8();
      if (lane < 16) { lds_st<float>((w * 32 + lane) * 4, C0[0]); lds_st<float>((w * 32 + 16 + lane) * 4, C1[0]); }
      BAR();
      sfor<8>([&](auto N) { cs[N] = lds_ld<float>((N * 16 + n16) * 4); }); };
    auto bounce = [&](auto I, auto SL) { constexpr int i = I; turn_a<SL * 2048>(Wa[i], Wa[i + 1], wrr, rd0, rd1); };
    auto publish_a = [&]() {
      st64_asm(__builtin_bit_cast(v2u, k.T), lane == 0 ? FLAG_OFF + FSTEP + (k.b * 4 + w) * 128 : OOB, k.rS);
      pin(wrv);
      float s = 0.f;
#pragma unroll
      for (int i = 0; i < 4; ++i) s += blo(wrv[i]) + bhi(wrv[i]);
      swr = wavesum(s); };
    auto first_a = [&]() {
      pin(fl);
      flag_wait(k, fl, vFA, 1);
      const unsigned abS = AB_OFF + jj * 524288 + (32 * k.kh + 8 * w) * 1024;
      sfor<12>([&](auto I) { constexpr int i = I; nx[i] = ld128(k.rS, vL, abS + ((i >> 3) ^ k.kh) * 65536 + (i & 7) * 1024); SB(); }); };
    piece(IC(0));
    piece(IC(1));
    piece(IC(2));
    piece(IC(3));
    piece(IC(4));
    piece(IC(5));
    piece(IC(6));
    piece(IC(7));
    piece(IC(8));
    piece(IC(9));
    piece(IC(10));
    piece(IC(11));
    piece(IC(12));
    piece(IC(13));
    piece(IC(14));
    piece(IC(15));
    piece(IC(16));
    piece(IC(17));
    piece(IC(18));
    piece(IC(19));
    pretouch();
    setup_rest();
    waitvm<9>();
    after_first_wait();
    p_m0(IC(20)); a1_mfma(IC(0), IC(0)); p_go(IC(20)); a1_refill(IC(0), IC(0));
    a1_mfma(IC(0), IC(1)); a1_refill(IC(0), IC(1));
    a1_mfma(IC(0), IC(2)); a1_refill(IC(0), IC(2));
    p_m0(IC(21)); a1_mfma(IC(0), IC(3)); p_go(IC(21)); a1_refill(IC(0), IC(3));
    a1_mfma(IC(0), IC(4)); a1_refill(IC(0), IC(4));
    a1_mfma(IC(0), IC(5)); a1_refill(IC(0), IC(5));
    p_m0(IC(22)); a1_mfma(IC(0), IC(6)); p_go(IC(22)); a1_refill(IC(0), IC(6));
    a1_mfma(IC(0), IC(7)); a1_refill(IC(0), IC(7));
    a1_mfma(IC(0), IC(8)); a1_refill(IC(0), IC(8));
    p_m0(IC(23)); a1_mfma(IC(0), IC(9)); p_go(IC(23)); a1_refill(IC(0), IC(9));
    a1_mfma(IC(0), IC(10)); a1_refill(IC(0), IC(10));
    a1_mfma(IC(0), IC(11)); a1_refill(IC(0), IC(11));
    p_m0(IC(24)); a1_mfma(IC(1), IC(0)); p_go(IC(24)); a1_refill(IC(1), IC(0));
    a1_mfma(IC(1), IC(1)); a1_refill(IC(1), IC(1));
    a1_mfma(IC(1), IC(2)); a1_refill(IC(1), IC(2));
    p_m0(IC(25)); a1_mfma(IC(1), IC(3)); p_go(IC(25)); a1_refill(IC(1), IC(3));
    a1_mfma(IC(1), IC(4)); a1_refill(IC(1), IC(4));
    a1_mfma(IC(1), IC(5)); a1_refill(IC(1), IC(5));
    p_m0(IC(26)); a1_mfma(IC(1), IC(6)); p_go(IC(26)); a1_refill(IC(1), IC(6));
    a1_mfma(IC(1), IC(7)); a1_refill(IC(1), IC(7));
    a1_mfma(IC(1), IC(8)); a1_refill(IC(1), IC(8));
    p_m0(IC(27)); a1_mfma(IC(1), IC(9)); p_go(IC(27)); a1_refill(IC(1), IC(9));
    a1_mfma(IC(1), IC(10)); a1_refill(IC(1), IC(10));
    a1_mfma(IC(1), IC(11)); a1_refill(IC(1), IC(11));
    p_m0(IC(28)); a1_mfma(IC(2), IC(0)); p_go(IC(28)); a1_refill(IC(2), IC(0));
    a1_mfma(IC(2), IC(1)); a1_refill(IC(2), IC(1));
    a1_mfma(IC(2), IC(2)); a1_refill(IC(2), IC(2));
    p_m0(IC(29)); a1_mfma(IC(2), IC(3)); p_go(IC(29)); a1_refill(IC(2), IC(3));
    a1_mfma(IC(2), IC(4)); a1_refill(IC(2), IC(4));
    a1_mfma(IC(2), IC(5)); a1_refill(IC(2), IC(5));
    p_m0(IC(30)); a1_mfma(IC(2), IC(6)); p_go(IC(30)); a1_refill(IC(2), IC(6));
    a1_mfma(IC(2), IC(7)); a1_refill(IC(2), IC(7));
    a1_mfma(IC(2), IC(8)); a1_refill(IC(2), IC(8));
    p_m0(IC(31)); a1_mfma(IC(2), IC(9)); p_go(IC(31)); a1_refill(IC(2), IC(9));
    a1_mfma(IC(2), IC(10)); a1_refill(IC(2), IC(10));
    a1_mfma(IC(2), IC(11)); a1_refill(IC(2), IC(11));
    waitvm<8>(); rdx(IC(1)); BAR();
    p_m0(IC(32)); a1_mfma(IC(3), IC(0)); p_go(IC(32)); a1_refill(IC(3), IC(0));
    a1_mfma(IC(3), IC(1)); a1_refill(IC(3), IC(1));
    a1_mfma(IC(3), IC(2)); a1_refill(IC(3), IC(2));
    p_m0(IC(33)); a1_mfma(IC(3), IC(3)); p_go(IC(33)); a1_refill(IC(3), IC(3));
    a1_mfma(IC(3), IC(4)); a1_refill(IC(3), IC(4));
    a1_mfma(IC(3), IC(5)); a1_refill(IC(3), IC(5));
    p_m0(IC(34)); a1_mfma(IC(3), IC(6)); p_go(IC(34)); a1_refill(IC(3), IC(6));
    a1_mfma(IC(3), IC(7)); a1_refill(IC(3), IC(7));
    a1_mfma(IC(3), IC(8)); a1_refill(IC(3), IC(8));
    p_m0(IC(35)); a1_mfma(IC(3), IC(9)); p_go(IC(35)); a1_refill(IC(3), IC(9));
    a1_mfma(IC(3), IC(10)); a1_refill(IC(3), IC(10));
    a1_mfma(IC(3), IC(11)); a1_refill(IC(3), IC(11));
    if constexpr (DIAG >= 2) STAMPC(32);
    p_m0(IC(36)); a1_mfma(IC(4), IC(0)); p_go(IC(36)); a1_refill(IC(4), IC(0));
    a1_mfma(IC(4), IC(1)); a1_refill(IC(4), IC(1));
    a1_mfma(IC(4), IC(2)); a1_refill(IC(4), IC(2));
    p_m0(IC(37)); a1_mfma(IC(4), IC(3)); p_go(IC(37)); a1_refill(IC(4), IC(3));
    a1_mfma(IC(4), IC(4)); a1_refill(IC(4), IC(4));
    a1_mfma(IC(4), IC(5)); a1_refill(IC(4), IC(5));
    p_m0(IC(38)); a1_mfma(IC(4), IC(6)); p_go(IC(38)); a1_refill(IC(4), IC(6));
    a1_mfma(IC(4), IC(7)); a1_refill(IC(4), IC(7));
    a1_mfma(IC(4), IC(8)); a1_refill(IC(4), IC(8));
    p_m0(IC(39)); a1_mfma(IC(4), IC(9)); p_go(IC(39)); a1_refill(IC(4), IC(9));
    a1_mfma(IC(4), IC(10)); a1_refill(IC(4), IC(10));
    a1_mfma(IC(4), IC(11)); a1_refill(IC(4), IC(11));
    p_m0(IC(40)); a1_mfma(IC(5), IC(0)); p_go(IC(40)); a1_refill(IC(5), IC(0));
    a1_mfma(IC(5), IC(1)); a1_refill(IC(5), IC(1));
    a1_mfma(IC(5), IC(2)); a1_refill(IC(5), IC(2));
    p_m0(IC(41)); a1_mfma(IC(5), IC(3)); p_go(IC(41)); a1_refill(IC(5), IC(3));
    a1_mfma(IC(5), IC(4)); a1_refill(IC(5), IC(4));
    a1_mfma(IC(5), IC(5)); a1_refill(IC(5), IC(5));
    p_m0(IC(42)); a1_mfma(IC(5), IC(6)); p_go(IC(42)); a1_refill(IC(5), IC(6));
    a1_mfma(IC(5), IC(7)); a1_refill(IC(5), IC(7));
    a1_mfma(IC(5), IC(8)); a1_refill(IC(5), IC(8));
    p_m0(IC(43)); a1_mfma(IC(5), IC(9)); p_go(IC(43)); a1_refill(IC(5), IC(9));
    a1_mfma(IC(5), IC(10)); a1_refill(IC(5), IC(10));
    a1_mfma(IC(5), IC(11)); a1_refill(IC(5), IC(11));
    p_m0(IC(44)); a1_mfma(IC(6), IC(0)); p_go(IC(44)); a1_refill(IC(6), IC(0));
    a1_mfma(IC(6), IC(1)); a1_refill(IC(6), IC(1));
    a1_mfma(IC(6), IC(2)); a1_refill(IC(6), IC(2));
    p_m0(IC(45)); a1_mfma(IC(6), IC(3)); p_go(IC(45)); a1_refill(IC(6), IC(3));
    a1_mfma(IC(6), IC(4)); wsl(IC(0)); a1_refill(IC(6), IC(4));
    a1_mfma(IC(6), IC(5)); a1_refill(IC(6), IC(5));
    p_m0(IC(46)); a1_mfma(IC(6), IC(6)); p_go(IC(46)); a1_refill(IC(6), IC(6));
    a1_mfma(IC(6), IC(7)); a1_refill(IC(6), IC(7));
    a1_mfma(IC(6), IC(8)); a1_refill(IC(6), IC(8));
    p_m0(IC(47)); a1_mfma(IC(6), IC(9)); p_go(IC(47)); a1_refill(IC(6), IC(9));
    a1_mfma(IC(6), IC(10)); wsl(IC(1)); a1_refill(IC(6), IC(10));
    a1_mfma(IC(6), IC(11)); a1_refill(IC(6), IC(11));
    waitvm<10>(); rdx(IC(2)); rdx(IC(3)); BAR();
    a1_mfma(IC(7), IC(0)); a1_refill(IC(7), IC(0));
    a1_mfma(IC(7), IC(1)); wsl(IC(2)); a1_refill(IC(7), IC(1));
    a1_mfma(IC(7), IC(2)); a1_refill(IC(7), IC(2));
    a1_mfma(IC(7), IC(3)); a1_refill(IC(7), IC(3));
    a1_mfma(IC(7), IC(4)); wsl(IC(3)); a1_refill(IC(7), IC(4));
    a1_mfma(IC(7), IC(5)); wsl(IC(4)); a1_refill(IC(7), IC(5));
    a1_mfma(IC(7), IC(6)); a1_refill(IC(7), IC(6));
    a1_mfma(IC(7), IC(7)); wsl(IC(5)); a1_refill(IC(7), IC(7));
    a1_mfma(IC(7), IC(8)); a1_refill(IC(7), IC(8));
    a1_mfma(IC(7), IC(9)); a1_refill(IC(7), IC(9));
    a1_mfma(IC(7), IC(10)); wsl(IC(6)); a1_refill(IC(7), IC(10));
    a1_mfma(IC(7), IC(11)); wsl(IC(7)); a1_refill(IC(7), IC(11));
    if constexpr (DIAG >= 2) STAMPC(33);
    a1_mfma(IC(8), IC(0)); a1_refill(IC(8), IC(0));
    a1_mfma(IC(8), IC(1)); a1_refill(IC(8), IC(1));
    a1_mfma(IC(8), IC(2)); a1_refill(IC(8), IC(2));
    a1_mfma(IC(8), IC(3)); a1_refill(IC(8), IC(3));
    a1_mfma(IC(8), IC(4)); wsl(IC(32)); a1_refill(IC(8), IC(4));
    a1_mfma(IC(8), IC(5)); a1_refill(IC(8), IC(5));
    a1_mfma(IC(8), IC(6)); a1_refill(IC(8), IC(6));
    a1_mfma(IC(8), IC(7)); a1_refill(IC(8), IC(7));
    a1_mfma(IC(8), IC(8)); a1_refill(IC(8), IC(8));
    a1_mfma(IC(8), IC(9)); a1_refill(IC(8), IC(9));
    a1_mfma(IC(8), IC(10)); wsl(IC(33)); a1_refill(IC(8), IC(10));
    a1_mfma(IC(8), IC(11)); a1_refill(IC(8), IC(11));
    a1_mfma(IC(9), IC(0)); a1_refill(IC(9), IC(0));
    a1_mfma(IC(9), IC(1)); a1_refill(IC(9), IC(1));
    a1_mfma(IC(9), IC(2)); a1_refill(IC(9), IC(2));
    a1_mfma(IC(9), IC(3)); a1_refill(IC(9), IC(3));
    a1_mfma(IC(9), IC(4)); a1_refill(IC(9), IC(4));
    a1_mfma(IC(9), IC(5)); a1_refill(IC(9), IC(5));
    a1_mfma(IC(9), IC(6)); a1_refill(IC(9), IC(6));
    a1_mfma(IC(9), IC(7)); a1_refill(IC(9), IC(7));
    a1_mfma(IC(9), IC(8)); a1_refill(IC(9), IC(8));
    a1_mfma(IC(9), IC(9)); a1_refill(IC(9), IC(9));
    a1_mfma(IC(9), IC(10)); a1_refill(IC(9), IC(10));
    a1_mfma(IC(9), IC(11)); a1_refill(IC(9), IC(11));
    a1_mfma(IC(10), IC(0)); a1_refill(IC(10), IC(0));
    a1_mfma(IC(10), IC(1)); a1_refill(IC(10), IC(1));
    a1_mfma(IC(10), IC(2)); a1_refill(IC(10), IC(2));
    a1_mfma(IC(10), IC(3)); a1_refill(IC(10), IC(3));
    a1_mfma(IC(10), IC(4)); a1_refill(IC(10), IC(4));
    a1_mfma(IC(10), IC(5)); a1_refill(IC(10), IC(5));
    a1_mfma(IC(10), IC(6)); a1_refill(IC(10), IC(6));
    a1_mfma(IC(10), IC(7)); a1_refill(IC(10), IC(7));
    a1_mfma(IC(10), IC(8)); a1_refill(IC(10), IC(8));
    a1_mfma(IC(10), IC(9)); a1_refill(IC(10), IC(9));
    a1_mfma(IC(10), IC(10)); a1_refill(IC(10), IC(10));
    a1_mfma(IC(10), IC(11)); a1_refill(IC(10), IC(11));
    waitvm<9>(); asm volatile("s_barrier" ::: "memory");
    a1_mfma(IC(11), IC(0)); a1_refill(IC(11), IC(0));
    a1_mfma(IC(11), IC(1)); a1_refill(IC(11), IC(1));
    a1_mfma(IC(11), IC(2)); a1_refill(IC(11), IC(2));
    a1_mfma(IC(11), IC(3)); a1_refill(IC(11), IC(3));
    a1_mfma(IC(11), IC(4)); a1_refill(IC(11), IC(4));
    a1_mfma(IC(11), IC(5)); a1_refill(IC(11), IC(5));
    a1_mfma(IC(11), IC(6)); a1_refill(IC(11), IC(6));
    a1_mfma(IC(11), IC(7)); a1_refill(IC(11), IC(7));
    a1_mfma(IC(11), IC(8)); a1_refill(IC(11), IC(8));
    a1_mfma(IC(11), IC(9)); a1_refill(IC(11), IC(9));
    a1_mfma(IC(11), IC(10)); a1_refill(IC(11), IC(10));
    a1_mfma(IC(11), IC(11)); a1_refill(IC(11), IC(11));
    if constexpr (DIAG >= 2) STAMPC(34);
    a1_mfma(IC(12), IC(0)); a1_refill(IC(12), IC(0));
    a1_mfma(IC(12), IC(1)); a1_refill(IC(12), IC(1));
    a1_mfma(IC(12), IC(2)); a1_refill(IC(12), IC(2));
    a1_mfma(IC(12), IC(3)); a1_refill(IC(12), IC(3));
    a1_mfma(IC(12), IC(4)); a1_refill(IC(12), IC(4));
    a1_mfma(IC(12), IC(5)); a1_refill(IC(12), IC(5));
    a1_mfma(IC(12), IC(6)); a1_refill(IC(12), IC(6));
    a1_mfma(IC(12), IC(7)); a1_refill(IC(12), IC(7));
    a1_mfma(IC(12), IC(8)); a1_refill(IC(12), IC(8));
    a1_mfma(IC(12), IC(9)); a1_refill(IC(12), IC(9));
    a1_mfma(IC(12), IC(10)); a1_refill(IC(12), IC(10));
    a1_mfma(IC(12), IC(11)); a1_refill(IC(12), IC(11));
    pattern_and_small_loads();
    a1_mfma(IC(13), IC(0)); a1_refill(IC(13), IC(0));
    a1_mfma(IC(13), IC(1)); a1_refill(IC(13), IC(1));
    a1_mfma(IC(13), IC(2)); a1_refill(IC(13), IC(2));
    a1_mfma(IC(13), IC(3)); a1_refill(IC(13), IC(3));
    a1_mfma(IC(13), IC(4)); a1_refill(IC(13), IC(4));
    a1_mfma(IC(13), IC(5)); a1_refill(IC(13), IC(5));
    a1_mfma(IC(13), IC(6)); a1_refill(IC(13), IC(6));
    a1_mfma(IC(13), IC(7)); a1_refill(IC(13), IC(7));
    a1_mfma(IC(13), IC(8)); a1_refill(IC(13), IC(8));
    a1_mfma(IC(13), IC(9)); a1_refill(IC(13), IC(9));
    a1_mfma(IC(13), IC(10)); a1_refill(IC(13), IC(10));
    a1_mfma(IC(13), IC(11)); a1_refill(IC(13), IC(11));
    a1_mfma(IC(14), IC(0)); a1_refill(IC(14), IC(0));
    a1_mfma(IC(14), IC(1)); a1_refill(IC(14), IC(1));
    a1_mfma(IC(14), IC(2)); a1_refill(IC(14), IC(2));
    a1_mfma(IC(14), IC(3)); a1_refill(IC(14), IC(3));
    a1_mfma(IC(14), IC(4)); a1_refill(IC(14), IC(4));
    a1_mfma(IC(14), IC(5)); a1_refill(IC(14), IC(5));
    a1_mfma(IC(14), IC(6)); a1_refill(IC(14), IC(6));
    a1_mfma(IC(14), IC(7)); a1_refill(IC(14), IC(7));
    a1_mfma(IC(14), IC(8)); a1_refill(IC(14), IC(8));
    a1_mfma(IC(14), IC(9)); a1_refill(IC(14), IC(9));
    a1_mfma(IC(14), IC(10)); a1_refill(IC(14), IC(10));
    a1_mfma(IC(14), IC(11)); a1_refill(IC(14), IC(11));
    a1_mfma(IC(15), IC(0)); a1_refill(IC(15), IC(0));
    a1_mfma(IC(15), IC(1)); a1_refill(IC(15), IC(1));
    a1_mfma(IC(15), IC(2)); a1_refill(IC(15), IC(2));
    a1_mfma(IC(15), IC(3)); a1_refill(IC(15), IC(3));
    a1_mfma(IC(15), IC(4)); a1_refill(IC(15), IC(4));
    a1_mfma(IC(15), IC(5)); a1_refill(IC(15), IC(5));
    a1_mfma(IC(15), IC(6)); a1_refill(IC(15), IC(6));
    a1_mfma(IC(15), IC(7)); a1_refill(IC(15), IC(7));
    a1_mfma(IC(15), IC(8)); a1_refill(IC(15), IC(8));
    a1_mfma(IC(15), IC(9)); a1_refill(IC(15), IC(9));
    a1_mfma(IC(15), IC(10)); a1_refill(IC(15), IC(10));
    a1_mfma(IC(15), IC(11)); a1_refill(IC(15), IC(11));
    if constexpr (DIAG >= 2) STAMPC(35);
    exchange_cs();
    a_stats<0>(k, S1, S2, mean, vari, rstd, sc); SB();
    a_emit1<0, 0>(k, U, cs, mean, sc);
    a_emit1<0, 1>(k, U, cs, mean, sc);
    a_emit1<0, 2>(k, U, cs, mean, sc);
    a_emit1<0, 3>(k, U, cs, mean, sc);
    STAMP(2);
    wsl(IC(34));
    wsl(IC(35));
    wsl(IC(36));
    wsl(IC(37));
    wsl(IC(38));
    wsl(IC(39));
    waitvm<29>();
    bounce(IC(0), IC(1));
    waitvm<27>();
    bounce(IC(2), IC(2));
    waitvm<25>();
    bounce(IC(4), IC(3));
    waitvm<23>();
    bounce(IC(6), IC(0));
    waitvm<21>();
    bounce(IC(32), IC(1));
    waitvm<6>();
    publish_a();
    wsl(IC(8));
    wsl(IC(9));
    wsl(IC(10));
    wsl(IC(11));
    wsl(IC(12));
    wsl(IC(13));
    waitvm<11>();
    bounce(IC(34), IC(2));
    if constexpr (DIAG >= 2) STAMPC(44);
    waitvm<9>();
    bounce(IC(36), IC(3));
    waitvm<7>();
    bounce(IC(38), IC(0));
    wsl(IC(14));
    wsl(IC(15));
    waitvm<6>();
    bounce(IC(8), IC(1));
    wsl(IC(16));
    wsl(IC(17));
    waitvm<6>();
    bounce(IC(10), IC(2));
    wsl(IC(18));
    wsl(IC(19));
    waitvm<6>();
    bounce(IC(12), IC(3));
    wsl(IC(20));
    wsl(IC(21));
    waitvm<6>();
    bounce(IC(14), IC(0));
    if constexpr (DIAG >= 2) STAMPC(45);
    wsl(IC(22));
    wsl(IC(23));
    waitvm<6>();
    bounce(IC(16), IC(1));
    wsl(IC(24));
    wsl(IC(25));
    waitvm<6>();
    bounce(IC(18), IC(2));
    wsl(IC(26));
    wsl(IC(27));
    waitvm<6>();
    bounce(IC(20), IC(3));
    wsl(IC(28));
    wsl(IC(29));
    waitvm<6>();
    bounce(IC(22), IC(0));
    ld64_asm(fl, vFA + FLAG_OFF + FSTEP, k.rS);
    wsl(IC(30));
    wsl(IC(31));
    waitvm<7>();
    bounce(IC(24), IC(1));
    wsl(IC(40));
    wsl(IC(41));
    waitvm<7>();
    bounce(IC(26), IC(2));
    if constexpr (DIAG >= 2) STAMPC(46);
    wsl(IC(42));
    wsl(IC(43));
    waitvm<7>();
    bounce(IC(28), IC(3));
    wsl(IC(44));
    wsl(IC(45));
    waitvm<6>();
    bounce(IC(30), IC(0));
    wsl(IC(46));
    wsl(IC(47));
    waitvm<6>();
    bounce(IC(40), IC(1));
    wsl(IC(48));
    wsl(IC(49));
    waitvm<6>();
    bounce(IC(42), IC(2));
    wsl(IC(50));
    wsl(IC(51));
    waitvm<6>();
    bounce(IC(44), IC(3));
    wsl(IC(52));
    wsl(IC(53));
    waitvm<6>();
    bounce(IC(46), IC(0));
    if constexpr (DIAG >= 2) STAMPC(47);
    first_a();
    wsl(IC(54));
    wsl(IC(55));
    waitvm<18>();
    bounce(IC(48), IC(1));
    wsl(IC(56));
    wsl(IC(57));
    waitvm<18>();
    bounce(IC(50), IC(2));
    wsl(IC(58));
    wsl(IC(59));
    waitvm<18>();
    bounce(IC(52), IC(3));
    wsl(IC(60));
    wsl(IC(61));
    waitvm<6>();
    bounce(IC(54), IC(0));
    wsl(IC(62));
    wsl(IC(63));
    waitvm<6>();
    bounce(IC(56), IC(1));
    waitvm<4>();
    bounce(IC(58), IC(2));
    if constexpr (DIAG >= 2) STAMPC(48);
    waitvm<2>();
    bounce(IC(60), IC(3));
    waitvm<0>();
    bounce(IC(62), IC(0));
    BAR();
    if (w == 0) piece(IC(4));
    if (w == 2) lds_st<v4u>(L_WR + ((((lane >> 4) ^ k.rh) * 4 + ((lane >> 2) & 3)) * 4 + (lane & 3)) * 16, wrv);
    if (w == 1 && lane < 16) { lds_st<f32x4>(L_CSUM + lane * 32, f32x4{cs[0], cs[1], cs[2], cs[3]}); lds_st<f32x4>(L_CSUM + lane * 32 + 16, f32x4{cs[4], cs[5], cs[6], cs[7]}); }
  }
  STAMP(3);
  unsigned wdn[8]; f32x4 cgu;
  Sel s1; V2 r1;
  v4u x1c[2], da[2][2][2], uu[2], wrc[2]; float x[16];
  unsigned wcol0, wcol1;
  auto phaseB = [&](auto LAYER) { constexpr int L = LAYER, RN = L == 0 ? RN1 : RN2;
    PHASE_LANE;
    unsigned vFA, vFY, vFme, vN, vY, wa1, wa2, wa3, ra, vGg, vGu, tfw, tfr, vXa, vXr, xbw, vWc, fhw, fhr, vPy, vZu, xbr[4];
    auto setup = [&](auto I) { constexpr int i = I;
      if constexpr (i == 0) wa1 = fresh(L_RED + ((w ^ 1) * 6 + 0) * 1024 + vL);
      if constexpr (i == 1) wa2 = fresh(L_RED + ((w ^ 2) * 6 + 2) * 1024 + vL);
      if constexpr (i == 2) wa3 = fresh(L_RED + ((w ^ 3) * 6 + 4) * 1024 + vL);
      if constexpr (i == 3) ra = fresh(L_RED + w * 6144 + vL);
      if constexpr (i == 4) vY = fresh(((8 * k.gB + ((q4 & 1) ^ tx)) * 1024 + (q4 >> 1) * 16 + n16) * 16 + 4 * k.tn * 1024);
      if constexpr (i == 5) vFY = fresh(flagY(n16, L == 0 ? w : jj >> 1));
      if constexpr (i == 6) vFme = fresh(flagMe(lane));
      if constexpr (L == 0) {
        if constexpr (i == 7) { tfw = fresh(L_FW + w * 1024 + vL); vN = vL; }
        if constexpr (i == 8) tfr = fresh(tr_addr(lane, L_FW + w * 1024));
      } else if constexpr (i == 7) {
        vFA = fresh(flagA(n16)); vN = fresh(yoff(crow0 + (lane & 7), 16 * w + (lane >> 3)));
        vXr = fresh((q4 * 64 + ((lane >> 2) & 3) * 16) * 16 + (lane & 3) * 4); xbw = fresh(L_XB + w * 256 + lane * 4); vWc = fresh((crow0 + (lane & 7)) * (S_ * 2));
        fhw = fresh(n16 == 0 ? L_FH + w * 64 + q4 * 16 : L_FW + w * 1024 + vL); fhr = fresh(L_FH + (lane & 7) * 4); vPy = fresh(fc >= 0 ? w * 512 + lane * 8 : OOB);
        vZu = fresh(((4 * ((n16 >> 2) ^ k.rh) + (n16 & 3)) * 4 + q4) * 32);
        sfor<4>([&](auto J) { xbr[J] = fresh(L_XB + w * 256 + (J ^ (mem & 3)) * 64 + q4 * 16); });
      } };
    const unsigned abS = AB_OFF + L * AB_SZ + jj * 524288 + (32 * k.kh + 8 * w) * 1024, abE = abS + k.kh * 65536, abO = abS + (1 - k.kh) * 65536;
    const unsigned yS = Y_OFF + L * Y_SZ + (k.c * 2 + k.kh) * YP, yE = yS + k.kh * 512, yO = yS + (1 - k.kh) * 512;
    const unsigned vL1 = fresh(vL + 1024), vL2 = fresh(vL + 2048), vL3 = fresh(vL + 3072);
    v4u ring[RN];
    auto lda = [&](auto N) { constexpr int n = N, ns = n >> 3, kk = n & 7;
      ring[n % RN] = ld128(k.rS, (kk & 3) == 0 ? vL : (kk & 3) == 1 ? vL1 : (kk & 3) == 2 ? vL2 : vL3, ((ns & 1) ? abO : abE) + (ns >> 1) * 131072 + (kk >> 2) * 4096); };
    auto ldn = [&](auto N) { constexpr int x = N >> 2, j = N & 3;
      if constexpr (L == 0) nx[N] = ld128(k.rS, vN, Y_OFF + j * YP + ((4 * k.gA + w) * 16 + 4 * (x ^ k.rh)) * 1024);
      else nx[N] = ld128(k.rS, vN, (x < 2 ? Y_OFF + x * 2048 : Y_OFF + Y_SZ) + j * YP); };
    f32x4 acc[8], accx[2], own[2][2], part[6], xc0; float sm[8]; unsigned pk[4];
    v2u fl; v4u rc[2], raw[4], py[2], xq[4], zu, zs; unsigned xr[4], xd[4], aold;
    v4u tf, dwf[4]; v2u xa; unsigned am, rowo, rowf; int aL; float av, c0, c1, c2, c3, sg1, xm1, gp[4], up[4]; f32x4 dd, dacc[4];
    if constexpr (L == 0) {
      sfor<12>([&](auto I) { ring[I] = nx[I]; });
      const unsigned vG = (128 * w + (lane & 31)) * (F_ * 2) + (fc >= 0 ? fc : mem) * 16; vGg = fresh(lane < 32 ? vG : OOB); vGu = fresh(lane < 32 ? OOB : vG);
      vXa = fresh((128 * k.gB + 16 * tx + n16) * 1024 + (128 * k.tn + 16 * (1 - k.kh) + 4 * q4) * 2);
    } else {
      sfor<8>([&](auto I) { setup(I); });
      if constexpr (DIAG >= 3) STAMP(54);
      RETRY_WHILE(anyl(maxv(maxv(0u, r1.v[0]), r1.v[1]) == POISON), (__builtin_amdgcn_s_sleep(GAP_R1), sfor<2>([&](auto I) { r1.v[I] = ld128(k.rS, vL, R1_OFF + k.xc * 2048 + I * 1024); })));
      BAR();
      if constexpr (DIAG >= 3) STAMP(55);
      sfor<8>([&](auto I) { lda(I); SB(); });
      if (w == 0) { const v4u b = asf(r1.v[1][0]) > asf(r1.v[0][0]) ? r1.v[1] : r1.v[0]; if (lane == best_lane(asf(b[0]))) lds_st<v4u>(L_ST + 64, b); }
      { const float m1 = lds_ld<float>(L_ST + (lane & 7) * 4), rs1 = lds_ld<float>(L_ST + 32 + (lane & 7) * 4);
        sfor<16>([&](auto E) { constexpr int e = E; const unsigned dw = x1c[e >> 3][(e >> 1) & 3]; const float v = (e & 1) ? bhi(dw) : blo(dw); x[e] = pinf(v + (v - m1) * rs1); }); }
      BAR();
      s1 = sel_of(lds_ld<v4u>(L_ST + 64), 0, 0);
      rowo = Y_OFF + (s1.row >> 4) * 16384 + (s1.row & 15) * 16;
      sfor<4>([&](auto I) { xr[I] = ld32(k.rS, vXr, rowo + w * 4096 + I * YP); });
      sg1 = pinf(sigmoidf(s1.r)); s1.rs = pinf(s1.rs);
      SB();
      sfor<4>([&](auto I) { lda(IC(8 + I)); SB(); });
    }
    auto dump = [&](auto T) { constexpr int tt = T; lds_st<f32x4>(((tt >> 1) == 1 ? wa1 : (tt >> 1) == 2 ? wa2 : wa3) + (tt & 1) * 1024, acc[tt]); };
    auto fetch1 = [&](auto I) { constexpr int i = I; part[i] = lds_ld<f32x4>(ra + i * 1024); };
    auto add1 = [&](auto J, auto HP) { constexpr int j = J, v = j & 7, st = j >> 3, hq = HP;
      sm[v] = pinf((st == 0 ? own[hq][v >> 2][v & 3] : sm[v]) + part[2 * st + (v >> 2)][v & 3]); };
    auto sty = [&](auto NS) { constexpr int ns = NS;
      auto e0 = __builtin_amdgcn_permlane16_swap(pk[0], pk[2], false, false), e1 = __builtin_amdgcn_permlane16_swap(pk[1], pk[3], false, false);
      st128(v4u{e0[0], e1[0], e0[1], e1[1]}, k.rS, vY, ((ns & 1) ? yO : yE) + (ns >> 1) * 1024); };
    STAMP(L == 0 ? 4 : 8);
    SB();
    sfor<8>([&](auto NS) { constexpr int ns = NS, h = ns & 1, rb = (ns * 8) % RN;
      sfor<64>([&](auto G) { constexpr int g = G, tt = ns == 7 ? ((g >> 3) + 2) & 7 : (g >> 2) & 7, kk = ns == 7 ? g & 7 : (g >> 5) * 4 + (g & 3);
        f32x4& d = tt < 2 ? own[h][tt & 1] : ns == 7 && tt >= 6 ? accx[tt - 6] : acc[tt];
        if constexpr (L == 0 && tt == 0 && kk == 0 && h == 1) mfac(d, ring[rb], Wa[0], xc0);
        else if constexpr (kk == 0) mfa0(d, ring[rb], Wa[tt * 8]); else mfa1(d, ring[rb + kk], Wa[tt * 8 + kk]);
        SB();
        if constexpr ((g & 7) == 5) { constexpr int j = g >> 3;
          if constexpr (RN == 24 && ns == 0) { lda(IC(12 + 2 * j)); }
          else { constexpr int n = 8 * ns + RN - 4 + j; if constexpr (n < 64) lda(IC(n)); }
          if constexpr (L == 0 && ns == 6 && j >= 4) ldn(IC(j - 4));
          if constexpr (L == 0 && ns == 7) ldn(IC(4 + j)); }
        if constexpr (RN == 24 && ns == 0 && (g & 7) == 1) lda(IC(13 + 2 * (g >> 3)));
        if constexpr (L == 0 && ns == 7 && (g & 7) == 1 && g >= 32) ldn(IC(12 + (g >> 3) - 4));
        if constexpr (ns < 7) {
          if constexpr (g >= 51 && (g & 3) == 3) dump(IC((g - 51) / 4 + 2));
          if constexpr (ns > 0) {
            if constexpr (g == 3) dump(IC(6));
            if constexpr (g == 7) dump(IC(7));
            if constexpr (g == 14) BAR();
            if constexpr (g >= 15 && g < 21) fetch1(IC(g - 15));
            if constexpr (g >= 26 && g < 50) add1(IC(g - 26), IC(1 - h));
            if constexpr (g >= 50 && g < 58) { constexpr int i = (g - 50) >> 1;
              if constexpr (((g - 50) & 1) == 0) pk[i] = pinu(cvtpk(sm[2 * i], sm[2 * i + 1])); else if constexpr (L == 1 && ns >= 5) pk[i] = pinu(san(pk[i])); }
            if constexpr (g == 44) BAR();
            if constexpr (g == 62) sty(IC(ns - 1));
          }
        } else {
          if constexpr (g == 3) dump(IC(6));
          if constexpr (g == 7) dump(IC(7));
          if constexpr (g == 11) BAR();
          if constexpr (g >= 12 && g < 18) fetch1(IC(g - 12));
          if constexpr (g >= 22 && g < 34) { add1(IC(2 * (g - 22)), IC(0)); add1(IC(2 * (g - 22) + 1), IC(0)); }
          if constexpr (g == 23) BAR();
          if constexpr (g == 24 || g == 28 || g == 32 || g == 39) dump(IC(g == 39 ? 5 : (g - 24) / 4 + 2));
          if constexpr (g >= 34 && g < 38) pk[g - 34] = pinu(L == 1 ? san(cvtpk(sm[2 * (g - 34)], sm[2 * (g - 34) + 1])) : cvtpk(sm[2 * (g - 34)], sm[2 * (g - 34) + 1]));
          if constexpr (g == 40) sty(IC(6));
          if constexpr (g == 47 || g == 55) lds_st<f32x4>(wa3 + (g == 55) * 1024, accx[g == 55]);
          if constexpr (g == 58) BAR();
          if constexpr (g >= 58 && g < 64) fetch1(IC(g - 58));
        }
        if constexpr (L == 0) {
          if constexpr (ns == 0 && g >= 32 && g < 50 && (g & 1) == 0) setup(IC((g - 32) >> 1));
          if constexpr (g == 59 && h == 1 && ns < 7) xa = __builtin_amdgcn_raw_buffer_load_b64(rX, vXa, ((ns + 1) >> 1) * 64, 0);
          if constexpr (g == 2 && ns == 0) xa = __builtin_amdgcn_raw_buffer_load_b64(rX, vXa, 0, 0);
          if constexpr (h == 0 && g >= 58 && g < 62) xc0[g - 58] = pinf((g & 1) ? bhi(xa[(g - 58) >> 1]) : blo(xa[(g - 58) >> 1]));
          if constexpr (ns == 0 && (g == 30 || g == 62))
            st128(splat(POISON), k.rS, (q4 & 1) * 65536 + (2 * k.gA + (w >> 1)) * 1024 + ((2 * (w & 1) + (q4 >> 1)) * 16 + n16) * 16, AB_OFF + AB_SZ + jj * 524288 + (g == 62) * 131072);
          if constexpr (g == 50 && (ns & 1) && ns >= 3) { waitvm<(ns == 7 ? 11 : ns == 5 && RN == 24 ? 4 : 6)>(); flag_set(k, vFme, (ns - 3) / 2 + 2); }
          if constexpr (g == 24 && (ns == 5 || ns == 7)) fl = flag_ld(k, vFY, ns == 5 ? 2 : 3);
          if constexpr (g == 10 && ns == 6) flag_wait(k, fl, vFY, 2);
          if constexpr (g == GG) graw[ns] = __builtin_amdgcn_raw_buffer_load_b128(mkrsrc(h ? P.wu : P.wg, D_ * F_ * 2), h ? vGu : vGg, ((ns >> 1) ^ (mem & 3)) * (32 * F_ * 2), (G_NT == 2 || (G_NT == 1 && ns < 2)) ? 2 : 0);
          if constexpr (h == 1 && ns >= 3) {
            constexpr int r = (ns - 3) >> 1;
            if constexpr (g == 8 || g == 9) { tf[2 * (g - 8)] = pinu(graw[2 * r][2 * (g - 8)] | graw[2 * r + 1][2 * (g - 8)]); tf[2 * (g - 8) + 1] = pinu(graw[2 * r][2 * (g - 8) + 1] | graw[2 * r + 1][2 * (g - 8) + 1]); }
            if constexpr (g == 11) lds_st<v4u>(tfw, tf);
            if constexpr (g == 22 || g == 23) { const v2u t = tr_read(tfr + (g - 22) * 64); wgu[r][2 * (g - 22)] = t[0]; wgu[r][2 * (g - 22) + 1] = t[1]; }
          }
          if constexpr (ns == 0 && g >= 21 && g < 25) { constexpr int i = g - 21; wdn[2 * i] = pinu(plo(dr[2 * i], dr[2 * i + 1])); wdn[2 * i + 1] = pinu(phi(dr[2 * i], dr[2 * i + 1])); }
        } else {
          if constexpr (RN == 16 ? (g == 24 && ns == 1) : (g == 40 && ns == 0)) fl = flag_ld(k, vFA, 6);
          if constexpr (RN == 16 ? (g == 12 && ns == 2) : (g == 36 && ns == 1)) flag_wait(k, fl, vFA, 6);
          if constexpr (g == 50 && ns == 5) { waitvm<(RN == 24 ? 4 : 6)>(); flag_set(k, vFme, 7); }
          if constexpr (g == 24 && ns == 7) fl = flag_ld(k, vFY, 7);
          if constexpr (g == 46 && ns < 4) {
            if (__builtin_expect(anyl(__builtin_isunordered(own[h][0][0], own[h][0][1]) || __builtin_isunordered(own[h][0][2], own[h][0][3])), 0)) {
              auto bad8 = [&]() { unsigned m = 0; sfor<8>([&](auto I) { m = maxv(m, ring[rb + I]); }); return anyl(m == POISON); };
              int spin = 0;
              _Pragma("unroll 1") do { fence_c(); sfor<8>([&](auto I) { lda(IC(8 * ns + I)); }); } while (bad8() && ++spin < SPIN_CAP);
              nop8();
              sfor<47>([&](auto G2) { constexpr int g2 = G2, t2 = (g2 >> 2) & 7, k2 = (g2 >> 5) * 4 + (g2 & 3);
                f32x4& d2 = t2 < 2 ? own[h][t2 & 1] : acc[t2];
                if constexpr (k2 == 0) mfa0(d2, ring[rb], Wa[t2 * 8]); else mfa1(d2, ring[rb + k2], Wa[t2 * 8 + k2]); });
              if constexpr (RN == 16 || ns > 0) sfor<2>([&](auto J) { lda(IC(8 * ns + RN + J)); });
            }
          }
          constexpr int T = ns * 64 + g;
          if constexpr (T == 4) flag_set(k, vFme, 6);
          if constexpr (T == 16) c0 = pinf(blo(xr[0]) + blo(xr[1]));
          if constexpr (T == 17) c1 = pinf(blo(xr[2]) + blo(xr[3]));
          if constexpr (T == 18) c2 = pinf(bhi(xr[0]) + bhi(xr[1]));
          if constexpr (T == 19) c3 = pinf(bhi(xr[2]) + bhi(xr[3]));
          if constexpr (T == 20) lds_st<unsigned>(xbw, cvtpk(c0 + c1, c2 + c3));
          if constexpr (T >= 22 && T < 26) { constexpr int i = T - 22; xq[i] = lds_ld<v4u>(xbr[i]); }
          if constexpr (T >= 28 && T < 32) mfv<T == 28>(dd, wgu[T - 28], xq[T - 28]);
          if constexpr (T == 39) { dd[0] = pinf(dd[0] - cgu[0] * s1.m); dd[1] = pinf(dd[1] - cgu[1] * s1.m); }
          if constexpr (T == 40) { dd[2] = pinf(dd[2] - cgu[2] * s1.m); dd[3] = pinf(dd[3] - cgu[3] * s1.m); }
          if constexpr (T == 41) lds_st<f32x4>(fhw, dd);
          if constexpr (T == 44) BAR();
          if constexpr (T >= 47 && T < 51) { gp[T - 47] = lds_ld<float>(fhr + (T - 47) * 64); up[T - 47] = lds_ld<float>(fhr + (T - 47) * 64 + 32); }
          if constexpr (T == 56) { c0 = pinf(gp[0] + gp[1]); c1 = pinf(gp[2] + gp[3]); }
          if constexpr (T == 57) c0 = pinf((c0 + c1) * s1.rs);
          if constexpr (T == 58) c1 = pinf(__builtin_amdgcn_exp2f(-1.44269504089f * c0));
          if constexpr (T == 60) { c2 = pinf(up[0] + up[1]); c3 = pinf(up[2] + up[3]); }
          if constexpr (T == 62) c1 = pinf(__builtin_amdgcn_rcpf(1.0f + c1));
          if constexpr (T == 64) c2 = pinf((c2 + c3) * s1.rs);
          if constexpr (T == 65) c0 = pinf(c0 * c1 * c2);
          if constexpr (T == 68) am = pinu(cvtpk(c0, dpp<0xB1>(c0)));
          if constexpr (T == 70) ffn_down8(k, am, wdn, vPy, PY_OFF + fc * 2048);
          if constexpr (T == 73) wcol0 = __builtin_amdgcn_raw_buffer_load_b16(mkrsrc(P.wsr, S_ * S_ * 2), vWc, s1.row * 2, 0);
          if constexpr (T == 75) wcol1 = __builtin_amdgcn_raw_buffer_load_b16(mkrsrc(P.wsi, S_ * S_ * 2), vWc, s1.row * 2, 0);
          if constexpr (T == 87) if (dc >= 0) sfor<4>([&](auto I) { xd[I] = ld32(k.rS, ((lane & 7) >> 1) * 4, rowo + (dc >> 2) * 1024 + (dc & 3) * 256 + I * YP); });
          if constexpr (T == 202) fl = flag_ld(k, 0, 6, rowf = (blk_of_A(k.c, k.tn, s1.row >> 6) * 4 + ((s1.row >> 4) & 3)) * 128);
          if constexpr (T == 267) RETRY_WHILE(flag_bad(k, fl), fl = flag_ld(k, 0, 6, rowf));
          if constexpr (ns * 64 + g == T_PL) if (dc >= 0) ffn_red_load(k, py, PY_OFF, dc);
          if constexpr (ns * 64 + g == T_PR) if (dc >= 0) ffn_red_partial(k, py, PY_OFF, dc);
          if constexpr (T == T_PL + 12) if (dc >= 0) { const unsigned x1 = comb1(xd); xm1 = pinf(((lane & 1) ? bhi(x1) : blo(x1)) - s1.m); }
          if constexpr (T >= T_PU - 7 && T < T_PU - 3) gp[T - T_PU + 7] = lds_ld<float>(L_FD + (((lane & 3) * 2 + ((lane >> 2) & 1)) * 4) + (T - T_PU + 7) * 32);
          if constexpr (T == T_PU) if (w == 0 && dc >= 0) {
            const float y = ((gp[0] + gp[1]) + (gp[2] + gp[3])) * sg1;
            const unsigned d = san(cvtpk(y, dpp<0xB1>(y)));
            st32(d, k.rS, !(lane & 1) ? (lane >> 3) * 2048 + (lane & 7) * 2 : OOB, U1_OFF + dc * 32);
            const float u = blo(cvtpk(y, 0.f));
            const float t = sum8(u * (q4 == 0 ? 1.0f : q4 == 1 ? u : xm1));
            const v4u st = {san(__builtin_amdgcn_readlane(asu(t), 0)), san(__builtin_amdgcn_readlane(asu(t), 16)), san(__builtin_amdgcn_readlane(asu(t), 32)), 0u};
            st128(st, k.rS, lane < 8 ? lane * 2048 + 16 : OOB, U1_OFF + dc * 32);
          }
          auto zld = [&]() { zu = ld128(k.rS, vZu, U1_OFF + k.xc * 2048); zs = ld128(k.rS, vL * 2 + 16, U1_OFF + k.xc * 2048); };
          if constexpr (T == T_UL) if (an >= 0) { zld();
            aold = __builtin_amdgcn_raw_buffer_load_b16(k.rS, n16 * 16, AB_OFF + AB_SZ + jj * 524288 + ((an * 64 + (s1.row >> 5)) * 64 + ((s1.row >> 3) & 3) * 16) * 16 + (s1.row & 7) * 2, SC1);
            sfor<4>([&](auto M) { constexpr int m = M; dwf[m] = lds_ld<v4u>(frag_addr(lane, m & 1) + w * 32768 + (m >> 1) * 16384 + an * 2048); });
            c3 = lds_ld<float>(L_CSUM + n16 * 32 + an * 4); }
          if constexpr (T == T_UM) if (an >= 0) {
            RETRY_WHILE(anyl(maxv(maxv(0u, zu), zs) == POISON), zld());
            SB();
            sfor<4>([&](auto M) { mfv0(dacc[M], zu, dwf[M]); });
            SB();
            const float t = rowsum(fold16(fold32(asf(zs[0]), asf(zs[1])), sum32(asf(zs[2]))));
            const float dm = asf(__builtin_amdgcn_readlane(asu(t), 0)) * (1.0f / D_), p3 = asf(__builtin_amdgcn_readlane(asu(t), 16)), p2 = asf(__builtin_amdgcn_readlane(asu(t), 32));
            const float gg = 1.0f + s1.rs, v0 = s1.var * gg * gg;
            c0 = pinf(__builtin_amdgcn_rsqf(fmaxf(v0 + (2.0f * gg * p3 + p2) * (1.0f / D_) - dm * dm, 0.f) + EPS));
            c1 = pinf((c0 * __builtin_amdgcn_sqrtf(v0 + EPS) - 1.0f) * blo(pinu(aold)));
            c0 = pinf(k.c ? -c0 : c0);
            c1 = pinf(c1 - c0 * dm * c3);
            SB(); sfor<4>([&](auto M) { pin(dacc[M]); });
            lds_st<float>(q4 == w ? L_FH + w * 64 + n16 * 4 : L_FW + w * 1024 + lane * 4, (dacc[0][0] + dacc[1][1]) + (dacc[2][2] + dacc[3][3]));
          }
          if constexpr (T >= T_UF - 10 && T < T_UF - 6) gp[T - T_UF + 10] = lds_ld<float>(L_FH + n16 * 4 + (T - T_UF + 10) * 64);
          if constexpr (T == T_UF) if (w == 0 && an >= 0) {
            const unsigned dl = san(asu(c1 + c0 * ((gp[0] + gp[1]) + (gp[2] + gp[3]))));
            sfor<2>([&](auto I) { st32(dl, k.rS, q4 * 4096 + n16 * 4, DA_OFF + I * 16384 + (k.c * 512 + 128 * k.tn + 16 * an) * 4); });
          }
        }
        SB();
      });
      if constexpr (DIAG >= 2) STAMPC(16 + L * 8 + ns);
    });
    sfor<8>([&](auto V) { constexpr int v = V; sm[v] = pinf(part[v >> 2][v & 3] + part[2 + (v >> 2)][v & 3]); });
    sfor<8>([&](auto V) { constexpr int v = V; sm[v] = pinf(sm[v] + part[4 + (v >> 2)][v & 3]); });
    sfor<8>([&](auto V) { constexpr int v = V; sm[v] = pinf(sm[v] + own[1][v >> 2][v & 3]); });
    sfor<4>([&](auto I) { pk[I] = san(cvtpk(sm[2 * I], sm[2 * I + 1])); });
    sty(IC(7));
    SB();
    flag_wait(k, fl, vFY, L == 0 ? 3 : 7);
    if constexpr (L == 1) sfor<4>([&](auto J) { ldn(IC(8 + J)); });
  };

  auto retouch = [&](int from) {
    const int l = fresh(k.lane);
    if (RETOUCH && l < 4) glds32(L_XB + w * 256, code0 + from + (((k.b >> 3) * 4 + w) * 4 + l) * 64); };
  retouch(16384);
  phaseB(IC(0));
  STAMP(5);
  if (RETOUCH == 2) retouch(16384);
  {
    PHASE_LANE;
    const unsigned vFA = fresh(flagA(n16)), vFY = fresh(flagY(n16, w)), vFme = fresh(flagMe(lane));
    f32x4 U[8], S1, S2, S3;
    const v4u ones = splat(0x3f803f80u);
    v4u yb[16][4];
    sfor<16>([&](auto I) { constexpr int i = I; yb[i >> 2][i & 3] = nx[i]; });
    const unsigned yrow = Y_OFF + (4 * k.gA + w) * 16384;
    unsigned qo[4]; sfor<4>([&](auto Q) { qo[Q] = yrow + (Q ^ k.rh) * 4096; });
    auto ldy = [&](auto I, auto J) { constexpr int i = I; yb[i][J] = ld128(k.rS, vL, qo[i & 3] + J * YP + (i >> 2) * 1024); };
    v2u fl, fa, fc5; const unsigned vFC = fresh(flagY(n16, jj >> 1));
    v4u wf[9], x1[2], pz;
    unsigned fb[2][2], pa[8], pb[8]; const unsigned wrb = fresh(L_WR + q4 * 16);
    float sx[8];
    sfor<4>([&](auto I) { constexpr int i = I; fb[i >> 1][i & 1] = fresh(frag_addr(lane, i & 1) + (i >> 1) * 65536); });
    auto rdw = [&](auto I, auto N) { constexpr int i = I, nn = N, p = i >> 2, q = i & 3;
      if constexpr (nn < 8) wf[nn] = lds_ld<v4u>(fb[q >> 1][p & 1] + ((q & 1) * 32768 + (p >> 1) * 16384 + nn * 2048));
      else wf[8] = lds_ld<v4u>(wrb + (q * 4 + p) * 64); };
    auto prm = [&](auto I, auto E) { constexpr int i = I, e = E;
      pa[e] = pinu((e & 1) ? phi(yb[i][0][e >> 1], yb[i][1][e >> 1]) : plo(yb[i][0][e >> 1], yb[i][1][e >> 1]));
      pb[e] = pinu((e & 1) ? phi(yb[i][2][e >> 1], yb[i][3][e >> 1]) : plo(yb[i][2][e >> 1], yb[i][3][e >> 1])); };
    auto dts = [&]() { sfor<8>([&](auto E) { sx[E] = dot2z(pa[E], ones[0]); }); sfor<8>([&](auto E) { sx[E] = dot2(pb[E], ones[0], sx[E]); }); };
    auto cvx = [&](auto I, auto E) { constexpr int i = I, e = E; x1[i & 1][e] = pinu(cvtpk(sx[2 * e], sx[2 * e + 1])); };
    float cs[8], dg; const float one = pinf(1.0f); f32x4 Q, mean, vari, rstd, sc;
    v4u cr[8]; const unsigned vC = fresh(yoff(crow0 + (lane & 7), 16 * w + (lane >> 3)));
    const unsigned vYp = fresh(((8 * k.gB + ((q4 & 1) ^ tx)) * 1024 + (q4 >> 1) * 16 + n16) * 16 + 4 * k.tn * 1024);
    sfor<4>([&](auto J) { ldy(IC(4), J); });
    sfor<9>([&](auto N) { rdw(IC(0), N); });
    sfor<8>([&](auto E) { prm(IC(0), E); }); SB(); dts(); SB();
    sfor<8>([&](auto E) { prm(IC(1), E); }); SB();
    sfor<4>([&](auto E) { cvx(IC(0), E); }); SB();
    sfor<16>([&](auto I) { constexpr int i = I; constexpr bool first = i == 0;
      if constexpr (i == 1) fl = flag_ld(k, vFY, 4);
      if constexpr (i == 2) { waitvm<13>(); flag_set(k, vFme, 5); }
      if constexpr (i == 3) flag_wait(k, fl, vFY, 4);
      if constexpr (i == 5) fl = flag_ld(k, vFY, 5);
      if constexpr (i == 7) flag_wait(k, fl, vFY, 5);
      if constexpr (i == 8) fc5 = flag_ld(k, vFC, 5);
      if constexpr (i == 10) fa = flag_ld(k, vFA, 5);
      if constexpr (i == 11) flag_wait(k, fc5, vFC, 5);
      if constexpr (i == PK0) pz = splat(POISON);
      SB();
      if constexpr (i < 15) dts();
      SB();
      sfor<11>([&](auto M) { constexpr int m = M;
        if constexpr (m >= 3) mfv<first>(U[m - 3], x1[i & 1], wf[m - 3]);
        else if constexpr (i < 15) { if constexpr (m == 0) mfv<first>(S1, x1[i & 1], ones); else if constexpr (m == 1) mfv<first>(S2, x1[i & 1], x1[i & 1]); else mfv<first>(S3, x1[i & 1], wf[8]); }
        SB();
        if constexpr (i == 14 && m == 10) { mfv1(S2, x1[1], x1[1]); mfv1(S1, x1[1], ones); mfv1(S3, x1[1], wf[8]); }
        if constexpr (i == 15) {
          if constexpr (m == 3) { const f32x4 c0 = lds_ld<f32x4>(L_CSUM + n16 * 32), c1 = lds_ld<f32x4>(L_CSUM + n16 * 32 + 16); sfor<4>([&](auto J) { cs[J] = c0[(int)J]; cs[4 + J] = c1[(int)J]; }); }
          if constexpr (m == 7) dg = a_diag(S2);
          if constexpr (m == 8) a_spread(Q, dg, one);
          if constexpr (m == 9) { mean = S1 * (1.0f / D_); pin(mean); }
        }
        if constexpr (m >= 2 && i < 15) rdw(IC(i + 1), IC(m == 2 ? 8 : m - 3));
        if constexpr (m >= 3 && m < 7 && i < 15) cvx(IC(i + 1), IC(m - 3));
        if constexpr (m < 2 && i < 14) { prm(IC(i + 2), IC(2 * m)); prm(IC(i + 2), IC(2 * m + 1)); }
        if constexpr (m >= 7 && i < 14) prm(IC(i + 2), IC(m - 3));
        if constexpr (m >= 7 && i + 5 < 16) ldy(IC(i + 5), IC(m - 7));
        if constexpr ((m == 7 || m == 9) && i >= 11 && i < 15) { constexpr int j = 2 * (i - 11) + (m == 9); cr[j] = ld128(k.rS, vC, Y_OFF + (j >> 2) * 2048 + (j & 3) * YP); }
        if constexpr (m == 10 && i >= PK0 && i < PK0 + 4) st128(pz, k.rS, vYp, Y_OFF + Y_SZ + (k.c * 2 + k.kh) * YP + 2048 + (i - PK0) * 512);
        SB(); });
      if constexpr (DIAG >= 2 && (i & 1)) STAMPC(36 + (i >> 1));
    });
    STAMP(6);
    asm volatile("s_nop 5" : "+v"(Q));
    a_scale<1>(k, Q, mean, vari, rstd, sc);
    sfor<R1POS>([&](auto I) { a_emit1<1, I>(k, U, cs, mean, sc); });
    if (w == (jj >> 1)) {
      if (!(jj & 1)) {
        float br = rstd[0] * (S3[0] - mean[0] * swr), bm = mean[0], bv = vari[0]; unsigned bi = 0;
#pragma unroll
        for (int r = 1; r < 4; ++r) { const float rv = rstd[r] * (S3[r] - mean[r] * swr); const bool g = rv > br; br = g ? rv : br; bm = g ? mean[r] : bm; bv = g ? vari[r] : bv; bi = g ? r : bi; }
        const unsigned long long bal = __builtin_amdgcn_ballot_w64(br == max32(max16(br)));
        rec_store(k, v4u{san(asu(br)), san(asu(bm)), san(asu(bv)), (unsigned)(64 * k.gA + 16 * w + 4 * q4 + bi)}, bal ? __builtin_ctzll(bal) : 0, true, k.gA * 4 + w, R1_OFF, 2048);
      }
    }
    if constexpr (DIAG >= 3) STAMP(52);
    sfor<4 - R1POS>([&](auto I) { a_emit1<1, I + R1POS>(k, U, cs, mean, sc); });
    if constexpr (DIAG >= 3) STAMP(53);

    if (w == (jj >> 1))
      if (n16 == 0 && (q4 >> 1) == (jj & 1)) { lds_st<f32x4>(L_ST + (q4 & 1) * 16, mean); lds_st<f32x4>(L_ST + 32 + (q4 & 1) * 16, rstd); }
    { const unsigned LO = fresh(0x00003f80), HI = fresh(0x3f800000);
      sfor<2>([&](auto I) { constexpr int i = I; float t[8] = {0.f, 0.f, 0.f, 0.f, 0.f, 0.f, 0.f, 0.f}; add4(t, cr[4 * i], cr[4 * i + 1], cr[4 * i + 2], cr[4 * i + 3], LO, HI);
        x1c[i] = v4u{cvtpk(t[0], t[1]), cvtpk(t[2], t[3]), cvtpk(t[4], t[5]), cvtpk(t[6], t[7])}; pin(x1c[i]); }); }
    flag_wait(k, fa, vFA, 5);
    if (DLY_AB) __builtin_amdgcn_s_sleep(DLY_AB);
    sfor<2>([&](auto I) { r1.v[I] = ld128(k.rS, w == 0 ? vL : OOB, R1_OFF + k.xc * 2048 + I * 1024); });
    SB();
    wgu[3] = to_frag(k, graw[6] | graw[7], L_FW + w * 1024);
    { const v4u on[4] = {ones, ones, ones, ones}; cgu = gemv<4>(wgu, on); }
  }
  STAMP(7);
  phaseB(IC(1));
  STAMP(9);
  {
    PHASE_LANE;
    const unsigned vC = fresh(yoff(crow0 + (lane & 7), 16 * w + (lane >> 3))), ch0 = 16 * w + (lane >> 3);
    V4 y2, y2b;
    auto ld_late = [&](V4& y) { sfor<4>([&](auto J) { y.v[J] = ld128(k.rS, vC, Y_OFF + Y_SZ + 2048 + J * YP); }); };
    auto bad4 = [&](const V4& y) { return anyl(maxv(maxv(maxv(maxv(0u, y.v[0]), y.v[1]), y.v[2]), y.v[3]) == POISON); };
    auto ld_small = [&]() {
      sfor<8>([&](auto I) { constexpr int i = I; da[i >> 2][(i >> 1) & 1][i & 1] = ld128(k.rS, ch0 * 32 + (i & 1) * 16, DA_OFF + k.xc * 4096 + (i >> 2) * 2048 + ((i >> 1) & 1) * 256); });
      sfor<2>([&](auto I) { uu[I] = ld128(k.rS, ch0 * 32, U1_OFF + k.xc * 2048 + I * 256); }); };
    ld_small();
    sfor<2>([&](auto I) { wrc[I] = __builtin_amdgcn_raw_buffer_load_b128(rWr, ch0 * 16, I * 128, 0); });
    if (DLY_C) __builtin_amdgcn_s_sleep(DLY_C);
    SB();
    ld_late(y2);
    SB();
    v4u x3[2]; float s = 0.f, qq = 0.f, dt = 0.f;
    const unsigned LO = fresh(0x00003f80), HI = fresh(0x3f800000), BOTH = fresh(0x3f803f80);
    auto fin = [&](int h) {
#pragma unroll
      for (int i = 0; i < 4; ++i) { const unsigned d = san(cvtpk(x[8 * h + 2 * i], x[8 * h + 2 * i + 1])); x3[h][i] = d; s = fdot(d, BOTH, s); qq = fdot(d, d, qq); dt = fdot(d, wrc[h][i], dt); } };
    const float w0 = blo(pinu(wcol0)), w1 = blo(pinu(wcol1));
    const unsigned slo = (unsigned)(crow0 + (lane & 7)) == s1.row ? LO : 0u, shi = slo << 16;
    auto bad_small = [&]() { unsigned m = 0; sfor<8>([&](auto I) { constexpr int i = I; m = maxv(m, da[i >> 2][(i >> 1) & 1][i & 1]); }); return anyl(maxv(maxv(m, uu[0]), uu[1]) == POISON); };
    RETRY_WHILE(bad_small(), ld_small());
#pragma unroll
    for (int e = 0; e < 16; ++e) x[e] = pinf(fdot(uu[e >> 3][(e >> 1) & 3], (e & 1) ? shi : slo, x[e] + w0 * asf(da[0][e >> 3][(e >> 2) & 1][e & 3]) + w1 * asf(da[1][e >> 3][(e >> 2) & 1][e & 3])));
    add4(x, nx[8], nx[9], nx[10], nx[11], LO, HI);
    fin(0);
    s = pinf(s); qq = pinf(qq); dt = pinf(dt); pin(x3[0]);
    RETRY2_WHILE(y2, y2b, bad4, ld_late, GAP_C);
    STAMP(10);
    add4(x + 8, y2.v[0], y2.v[1], y2.v[2], y2.v[3], LO, HI);
    fin(1);
    { float t = fold16(fold32(s, qq), sum32(dt)); t += dpp<0x128>(t);
      if (n16 < 8 && q4 < 3) lds_st<float>(L_CS + (lane & 7) * 64 + q4 * 16 + w * 4, t); }
    BAR();
    const f32x4 t0 = lds_ld<f32x4>(L_CS + (lane & 7) * 64), t1 = lds_ld<f32x4>(L_CS + (lane & 7) * 64 + 16), t2 = lds_ld<f32x4>(L_CS + (lane & 7) * 64 + 32);
    const float m3 = ((t0[0] + t0[1]) + (t0[2] + t0[3])) * (1.0f / D_), v3 = fmaxf(((t2[0] + t2[1]) + (t2[2] + t2[3])) * (1.0f / D_) - m3 * m3, 0.f);
    const float rs3 = __builtin_amdgcn_rsqf(v3 + EPS), rv = rs3 * (((t1[0] + t1[1]) + (t1[2] + t1[3])) - m3 * swr);
    float mx = fmx(rv, dpp<0xB1>(rv)); mx = fmx(mx, dpp<0x4E>(mx)); mx = fmx(mx, dpp<0x141>(mx));
    const unsigned bal = (unsigned)__builtin_amdgcn_ballot_w64(rv == mx) & 0xffu;
    const int best = bal ? __builtin_ctz(bal) : 0;
    rec_store(k, v4u{san(asu(rv)), san(asu(m3)), san(asu(v3)), (unsigned)(crow0 + best)}, best, w == 0, k.b, R2_OFF, 4096);
    sfor<2>([&](auto I) { st128(x3[I], k.rS, (lane & 7) == best ? k.b * 1024 + ch0 * 16 : OOB, XR_OFF + I * 128); });
    SB();
    v4u o[2];
#pragma unroll
    for (int i = 0; i < 8; ++i) { const float a = blo(x3[i >> 2][i & 3]), b = bhi(x3[i >> 2][i & 3]); o[i >> 2][i & 3] = pinu(cvtpk(a + (a - m3) * rs3, b + (b - m3) * rs3)); }
    const rsrc_t rO = mkrsrc(P.out, S_ * D_ * 2);
    const unsigned vO = (crow0 + (lane & 7)) * 1024 + ch0 * 16;
    sfor<2>([&](auto I) { __builtin_amdgcn_raw_buffer_store_b128(o[I], rO, (lane & 7) != best ? vO : OOB, I * 128, OUT_AUX); });
    STAMP(11);
    __builtin_amdgcn_s_sleep(DLY_R2);
    v4u rc, rcb;
    auto ld_rc = [&](v4u& r) { r = ld128(k.rS, vL, R2_OFF + k.xc * 4096 + w * 1024); };
    auto bad1 = [&](const v4u& r) { return anyl(maxv(0u, r) == POISON); };
    ld_rc(rc);
    RETRY2_WHILE(rc, rcb, bad1, ld_rc, GAP_R2);
    if (lane == best_lane(asf(rc[0]))) { rc[3] |= (w * 64 + lane) << 16; lds_st<v4u>(L_XB + 512 + w * 16, rc); }
    BAR();
    rc = lds_ld<v4u>(L_XB + 512 + (lane & 3) * 16);
    float mr = fmx(asf(rc[0]), dpp<0xB1>(asf(rc[0]))); mr = fmx(mr, dpp<0x4E>(mr));
    const unsigned bw = (unsigned)__builtin_amdgcn_ballot_w64(asf(rc[0]) == mr) & 15u;
    const int Lw = bw ? __builtin_ctz(bw) : 0;
    const unsigned xro = XR_OFF + (__builtin_amdgcn_readlane(rc[3], Lw) >> 16) * 1024;
    v4u xq[4];
    auto ld_row = [&]() { sfor<4>([&](auto I) { xq[I] = ld128(k.rS, w * 256 + q4 * 16, xro + (I ^ (mem & 3)) * 64); }); };
    ld_row();
    const int oc = k.xc * 32 + (k.b >> 3);
    unsigned xd = ld32(k.rS, 0, xro + oc * 4);
    const Sel s2 = sel_of(rc, 0, Lw);
    STAMP(12);
    sfor<2>([&](auto I) { __builtin_amdgcn_raw_buffer_store_b128(o[I], rO, ((lane & 7) == best && (unsigned)(crow0 + best) != s2.row) ? vO : OOB, I * 128, OUT_AUX); });
    RETRY_WHILE(anyl(maxv(maxv(maxv(maxv(0u, xq[0]), xq[1]), xq[2]), xq[3]) == POISON), ld_row());
    { const f32x4 dd = gemv<4>(wgu, xq); if (n16 == 0) lds_st<f32x4>(L_FH + w * 64 + q4 * 16, dd - cgu * s2.m); }
    BAR();
    ffn_mid(k, s2, wdn, PY_OFF + PY_SZ, fc);
    STAMP(13);
    RETRY_WHILE(anyl(xd == POISON), xd = ld32(k.rS, 0, xro + oc * 4));
    const float sg = pinf(sigmoidf(s2.r)), b0 = pinf(blo(xd) + (blo(xd) - s2.m) * s2.rs), b1 = pinf(bhi(xd) + (bhi(xd) - s2.m) * s2.rs);
    const unsigned vP = fresh(lane < 56 ? (56 * w + lane) * 2048 : OOB);
    __builtin_amdgcn_s_sleep(DLY_PY);
    v2u py, pyb;
    auto ld_py = [&](v2u& p) { p = ld64(k.rS, vP, PY_OFF + PY_SZ + oc * 8); };
    auto bad2 = [&](const v2u& p) { return anyl(max(p[0], p[1]) == POISON); };
    ld_py(py);
    RETRY2_WHILE(py, pyb, bad2, ld_py, GAP_PY);
    { const float t = rowsum(sum16(fold32(asf(py[0]), asf(py[1])))); if ((lane & 31) == 0) lds_st<float>(L_FD + (lane >> 5) * 16 + w * 4, t); }
    BAR();
    if (w == 0) {
      const f32x4 p0 = lds_ld<f32x4>(L_FD), p1 = lds_ld<f32x4>(L_FD + 16);
      __builtin_amdgcn_raw_buffer_store_b32(cvtpk(b0 + ((p0[0] + p0[1]) + (p0[2] + p0[3])) * sg, b1 + ((p1[0] + p1[1]) + (p1[2] + p1[3])) * sg), rO, lane == 0 ? oc * 4 : OOB, s2.row * 1024, 0);
    }
    if (k.b == 0 && w == 0 && lane == 0) st64(__builtin_bit_cast(v2u, k.T), k.rS, 0, 0);
  }
  STAMP(14);
}
}

extern "C" void kernel_launch(void* const* d_in, const int* in_sizes, int n_in, void* d_out, int out_size, void* d_ws, size_t ws_size, hipStream_t stream) {
  Params P;
  P.x0 = (const u16*)d_in[0]; P.wsr = (const u16*)d_in[1]; P.wsi = (const u16*)d_in[2]; P.wdr = (const u16*)d_in[3]; P.wdi = (const u16*)d_in[4];
  P.wr = (const u16*)d_in[6]; P.wg = (const u16*)d_in[7]; P.wu = (const u16*)d_in[8]; P.wdn = (const u16*)d_in[9];
  P.out = (u16*)d_out; P.ws = (char*)d_ws;
  (void)hipFuncSetAttribute((const void*)fnet_mod_kernel, hipFuncAttributeMaxDynamicSharedMemorySize, LDS_BYTES);
  fnet_mod_kernel<<<256, 256, LDS_BYTES, stream>>>(P);
}
```
